```python
import torch

def _softmax(x, axis=-1):
    return torch.softmax(x, dim=axis)
def _gelu(x):
    return torch.nn.functional.gelu(x, approximate="tanh")
def _silu(x):
    return torch.nn.functional.silu(x)
def _sigmoid(x):
    return torch.sigmoid(x)
def _layernorm(x, axis=-1, eps=1e-5):
    m = x.mean(dim=axis, keepdim=True)
    v = x.var(dim=axis, keepdim=True, unbiased=False)
    return (x - m) / torch.sqrt(v + eps)
def _scatter_add(tgt, idx, src, axis=0):
    return tgt.clone().index_add_(axis, idx, src)
def _segment_sum(src, seg, n):
    out = torch.zeros((n,) + src.shape[1:], dtype=src.dtype)
    return out.index_add_(0, seg, src)
def _pad(x, pw):
    flat = tuple(v for pair in reversed(pw) for v in pair)
    return torch.nn.functional.pad(x, flat)

kernel_name = 'parallel_sum__sliding_block__glu_gated_glu__prompt'

def setup_inputs():

    _w = (lambda a: (lambda t: t.to(torch.bfloat16) if t.is_floating_point() else t)(torch.as_tensor(a)))
    def _A_setup():
        B, S, D = 1, 2048, 640
        return {
            "x": _w((lambda *s: torch.randn(*s, dtype=torch.bfloat16) * 0.05)(B, S, D)),
            "wqkv": _w((lambda *s: torch.randn(*s, dtype=torch.bfloat16) * 0.05)(D, 3 * D)),
            "wo": _w((lambda *s: torch.randn(*s, dtype=torch.bfloat16) * 0.05)(D, D)),
        }
    _A_in = _A_setup()

    def _B_setup():
        B, S, D, F = 1, 2048, 640, 1536
        return {
            "x": _w((lambda *s: torch.randn(*s, dtype=torch.bfloat16) * 0.05)(B, S, D)),
            "w_gate": _w((lambda *s: torch.randn(*s, dtype=torch.bfloat16) * 0.05)(D, F)),
            "w_up": _w((lambda *s: torch.randn(*s, dtype=torch.bfloat16) * 0.05)(D, F)),
            "w_down": _w((lambda *s: torch.randn(*s, dtype=torch.bfloat16) * 0.05)(F, D)),
        }
    _B_in = _B_setup()

    return {
        "A_x": _A_in["x"],
        "A_wqkv": _A_in["wqkv"],
        "A_wo": _A_in["wo"],
        "B_x": _B_in["x"],
        "B_w_gate": _B_in["w_gate"],
        "B_w_up": _B_in["w_up"],
        "B_w_down": _B_in["w_down"],
    }

def reference(A_x, A_wqkv, A_wo, B_x, B_w_gate, B_w_up, B_w_down):
    def _A_ref(x, wqkv, wo):
        B, S, D = x.shape
        H = 32; Dh = D // H; W = 256
        qkv = (lambda x, *d: x.permute(*d))((x @ wqkv).reshape(B, S, 3, H, Dh), 2, 0, 3, 1, 4)
        q, k, v = qkv[0], qkv[1], qkv[2]
        scores = torch.einsum("bhqd,bhkd->bhqk", q, k) * (Dh ** -0.5)
        idx = torch.arange(S)
        mask = (idx[:, None] - idx[None, :] >= 0) & (idx[:, None] - idx[None, :] < W)
        scores = torch.where(mask[None, None], scores, -1e9)
        p = _softmax(scores, axis=-1)
        o = torch.einsum("bhqk,bhkd->bhqd", p, v)
        return (lambda x, *d: x.permute(*d))(o, 0, 2, 1, 3).reshape(B, S, D) @ wo

    def _B_ref(x, w_gate, w_up, w_down):
        g = torch.einsum("bsd,df->bsf", x, w_gate)
        u = torch.einsum("bsd,df->bsf", x, w_up)
        h = _sigmoid(g) * u
        return torch.einsum("bsf,fd->bsd", h, w_down)

    x = A_x
    xn = _layernorm(x, axis=-1)
    return x + _A_ref(xn, A_wqkv, A_wo) + _B_ref(xn, B_w_gate, B_w_up, B_w_down)
```

```cpp
#include <hip/hip_runtime.h>
#include <hip/hip_bf16.h>
#include <stdint.h>


typedef __attribute__((ext_vector_type(2))) __bf16 bf16x2;
typedef __attribute__((ext_vector_type(4))) __bf16 bf16x4;
typedef __attribute__((ext_vector_type(8))) __bf16 bf16x8;
typedef __attribute__((ext_vector_type(4))) short s16x4;
typedef __attribute__((ext_vector_type(2))) float f32x2;
typedef __attribute__((ext_vector_type(4))) float f32x4;
typedef __attribute__((ext_vector_type(16))) float f32x16;
typedef __attribute__((ext_vector_type(2))) unsigned u32x2;
typedef __attribute__((ext_vector_type(4))) unsigned u32x4;
#define LAS __attribute__((address_space(3)))
#define DI __device__ __forceinline__

constexpr size_t QKV_OFF = 0;
constexpr size_t A2_OFF = 8u << 20;
constexpr size_t B2_OFF = 17u << 20;

constexpr float LOG2E = 1.4426950408889634f;
constexpr float QSCALE = 0.22360679774997896f * LOG2E;

DI void dma16(unsigned voff, const void* sbase, unsigned lds) {
  asm volatile("s_mov_b32 m0, %2\n\ts_nop 0\n\tglobal_load_lds_dwordx4 %0, %1"
               :: "v"(voff), "s"(sbase), "s"(lds) : "memory");
}
template <int N> DI void wait_vm() { asm volatile("s_waitcnt vmcnt(%0)" :: "i"(N) : "memory"); }
template <int N, int M> DI void wait_vm_lgkm() { asm volatile("s_waitcnt vmcnt(%0) lgkmcnt(%1)" :: "i"(N), "i"(M) : "memory"); }
template <int M> DI void wait_lgkm() { asm volatile("s_waitcnt lgkmcnt(%0)" :: "i"(M) : "memory"); }
DI void barrier() { __builtin_amdgcn_s_barrier(); asm volatile("" ::: "memory"); }

DI bf16x8 lds_rd128(unsigned a) { return *(LAS bf16x8*)(size_t)a; }
DI f32x4 lds_rdf4(unsigned a) { return *(LAS f32x4*)(size_t)a; }
DI bf16x4 lds_tr(unsigned a) { return __builtin_amdgcn_ds_read_tr16_b64_v4bf16((LAS bf16x4*)(size_t)a); }
DI bf16x8 lds_trfrag(unsigned a, unsigned step) {
  bf16x4 lo = lds_tr(a), hi = lds_tr(a + step);
  return __builtin_shufflevector(lo, hi, 0, 1, 2, 3, 4, 5, 6, 7);
}
DI unsigned pk2(float a, float b) {
  f32x2 v = {a, b};
  return __builtin_bit_cast(unsigned, __builtin_convertvector(v, bf16x2));
}
DI float bfr(float a) { return (float)(__bf16)a; }
DI float lo_f(unsigned u) { return __uint_as_float(u << 16); }
DI float hi_f(unsigned u) { return __uint_as_float(u & 0xffff0000u); }
DI f32x16 mfma32(bf16x8 a, bf16x8 b, f32x16 c) { return __builtin_amdgcn_mfma_f32_32x32x16_bf16(a, b, c, 0, 0, 0); }

constexpr unsigned K1_WSLOT = 20480, K1_XOFF = 5 * K1_WSLOT, K1_XSLOT = 16384;
constexpr unsigned K1_EX = K1_XOFF + 3 * K1_XSLOT;
constexpr unsigned K1_LDS = 163840;

DI void acc_sum(float& s0, float& s1, bf16x8 f) {
  u32x4 u = __builtin_bit_cast(u32x4, f);
#pragma unroll
  for (int i = 0; i < 4; ++i) { s0 += lo_f(u[i]); s1 += hi_f(u[i]); }
}
DI void acc_stats(float& s, float& q, bf16x8 f) {
  u32x4 u = __builtin_bit_cast(u32x4, f);
#pragma unroll
  for (int i = 0; i < 4; ++i) {
    float a = lo_f(u[i]), b = hi_f(u[i]);
    s += a; q = __builtin_fmaf(a, a, q);
    s += b; q = __builtin_fmaf(b, b, q);
  }
}

template <int T>
DI void k1_body(const char* __restrict__ x, const char* __restrict__ wqkv, const char* __restrict__ wg,
                const char* __restrict__ wu, char* __restrict__ ws, int p, int odd, int mt, int w, int lane) {
  constexpr int NT = T == 0 ? 3 : 2;
  constexpr int NW = T == 0 ? 2 : 3;
  constexpr int G = NW + 1;
  const int wm = w & 1, nlo = (w >> 1) & 1, rot = wm ^ nlo, v = w & 3;
  const int h = lane >> 5, n = lane & 31;

  const char* wb[NW]; unsigned wv[NW]; unsigned wstep[NW];
  {
    const int r = lane >> 3, u = lane & 7;
#pragma unroll
    for (int j = 0; j < NW; ++j) {
      const int lc = T == 0 ? 3 + j : j;
      bool isq; int unit; int up;
      if (T == 0) { isq = false; unit = odd ? 3 * p + 2 : 3 * p + 1; up = j; }
      else if (odd) { isq = true; unit = 4 * p + 1 + j; if (unit > 29) unit = 29; up = 0; }
      else { isq = (j == 0); unit = (j == 0) ? 4 * p : 3 * p; up = (j == 2); }
      (void)lc;
      const unsigned stride = isq ? 3840u : 3072u;
      wb[j] = (isq ? wqkv : (up ? wu : wg)) + unit * 128 + (size_t)(8 * v) * stride;
      wstep[j] = 32 * stride;
      wv[j] = r * stride + ((u ^ (((r >> 1) & 1) << 2) ^ (up << 1)) << 4);
    }
  }
  const char* xb = x + (size_t)(128 * mt + 8 * w) * 1280;
  unsigned xv;
  { const int r = lane >> 3, u = lane & 7; xv = r * 1280 + ((u ^ (4 * (w & 1) + (r >> 1))) << 4); }
  const unsigned wdst = (T == 0 ? 3 * 4096 : 0) + v * 1024;
  const unsigned xdst = K1_XOFF + w * 1024;

  auto issueW = [&](unsigned slotoff) {
#pragma unroll
    for (int j = 0; j < NW; ++j) { dma16(wv[j], wb[j], slotoff + wdst + j * 4096); wb[j] += wstep[j]; }
  };
  auto issueX = [&](int stage, int part, unsigned slotoff) {
    dma16(xv, xb + part * (64 * 1280) + stage * 128, slotoff + xdst + part * 8192);
  };

  unsigned wa[NT];
  {
    const int cb = (lane >> 4) & 1, q = (lane >> 2) & 3, pp = lane & 3;
    const unsigned rowoff = (8 * h + q) * 128 + (pp & 1) * 8 + rot * 2048;
    const int sw = (q >> 1) << 2;
#pragma unroll
    for (int i = 0; i < NT; ++i) {
      bool isq; int lc, sel;
      if (T == 1) { isq = false; lc = 3; sel = 2 * nlo + i; }
      else if (odd) { isq = true; int j = 3 * nlo + i; lc = j >> 1; sel = j & 1; }
      else { isq = (i == 0); lc = (i == 0) ? 0 : 1; sel = (i == 0) ? nlo : 2 * nlo + i - 1; }
      unsigned a;
      if (isq) a = lc * 4096 + (((4 * sel + 2 * cb + (pp >> 1)) ^ sw) << 4);
      else a = (lc + cb) * 4096 + (((2 * sel + (pp >> 1)) ^ sw ^ (cb << 1)) << 4);
      wa[i] = a + rowoff;
    }
  }
  const int wd = rot ? -2048 : 2048;
  unsigned xa[2][2];
  {
    const int row = 64 * wm + n, sw = (row >> 1) & 7;
#pragma unroll
    for (int c = 0; c < 2; ++c)
#pragma unroll
      for (int j = 0; j < 2; ++j)
        xa[c][j] = K1_XOFF + row * 128 + (((4 * c + 2 * (j ^ rot) + h) ^ sw) << 4);
  }

  f32x16 acc[NT][2];
#pragma unroll
  for (int i = 0; i < NT; ++i) { acc[i][0] = (f32x16)(0.f); acc[i][1] = (f32x16)(0.f); }
  float cs0[NT], cs1[NT];
#pragma unroll
  for (int i = 0; i < NT; ++i) { cs0[i] = 0.f; cs1[i] = 0.f; }
  float rs[2] = {0.f, 0.f}, rq[2] = {0.f, 0.f};

  issueX(0, 0, 0);
  issueW(0 * K1_WSLOT); issueX(0, 1, 0);
  issueW(1 * K1_WSLOT); issueX(1, 0, K1_XSLOT);
  issueW(2 * K1_WSLOT); issueX(1, 1, K1_XSLOT);
  issueW(3 * K1_WSLOT); issueX(2, 0, 2 * K1_XSLOT);
  wait_vm<3 * G>();
  barrier();
  bf16x8 xf[2][2];
  bf16x8 wf0[NT];
#pragma unroll
  for (int j = 0; j < 2; ++j) { xf[j][0] = lds_rd128(xa[0][j]); xf[j][1] = lds_rd128(xa[0][j] + 4096); }
#pragma unroll
  for (int i = 0; i < NT; ++i) wf0[i] = lds_trfrag(wa[i], 512);

  unsigned wrd = 0;
  unsigned wwr = 4 * K1_WSLOT;
  unsigned xrd = 0;
  unsigned xwr = 2 * K1_XSLOT;

#pragma unroll 1
  for (int it = 0; it < 10; ++it) {
#pragma unroll
    for (int c = 0; c < 2; ++c) {
      if (c == 0) {
        if (it < 8) wait_vm_lgkm<2 * G, 2 * NT>();
        else if (it == 8) wait_vm_lgkm<2 * G - 1, 2 * NT>();
        else wait_vm_lgkm<0, 2 * NT>();
      } else {
        if (it < 8) wait_vm_lgkm<2 * G, 2 * NT>();
        else wait_vm_lgkm<NW, 2 * NT>();
      }
      barrier();
      const unsigned xnext = xrd == 2 * K1_XSLOT ? 0 : xrd + K1_XSLOT;
      if (it < 8) {
        issueW(wwr);
        if (c == 0) issueX(it + 2, 1, xwr);
        else if (it < 7) issueX(it + 3, 0, xrd);
      }
      wwr = wwr == 4 * K1_WSLOT ? 0 : wwr + K1_WSLOT;
      const unsigned wnext = wrd == 4 * K1_WSLOT ? 0 : wrd + K1_WSLOT;
      bf16x8 xn[2][2], wf1[NT], wn[NT];
#pragma unroll
      for (int i = 0; i < NT; ++i) wf1[i] = lds_trfrag(wa[i] + wrd + wd, 512);
      {
        const unsigned xo = c == 0 ? xrd : xnext;
#pragma unroll
        for (int j = 0; j < 2; ++j) {
          xn[j][0] = lds_rd128(xa[c ^ 1][j] + xo);
          xn[j][1] = lds_rd128(xa[c ^ 1][j] + xo + 4096);
        }
      }
#pragma unroll
      for (int i = 0; i < NT; ++i) {
        acc[i][0] = mfma32(wf0[i], xf[0][0], acc[i][0]);
        acc[i][1] = mfma32(wf0[i], xf[0][1], acc[i][1]);
        acc_sum(cs0[i], cs1[i], wf0[i]);
      }
      if (c == T) { acc_stats(rs[0], rq[0], xf[0][0]); acc_stats(rs[1], rq[1], xf[0][1]); }
      asm volatile("" ::: "memory");
#pragma unroll
      for (int i = 0; i < NT; ++i) wn[i] = lds_trfrag(wa[i] + wnext, 512);
#pragma unroll
      for (int i = 0; i < NT; ++i) {
        acc[i][0] = mfma32(wf1[i], xf[1][0], acc[i][0]);
        acc[i][1] = mfma32(wf1[i], xf[1][1], acc[i][1]);
      }
#pragma unroll
      for (int j = 0; j < 2; ++j) { xf[j][0] = xn[j][0]; xf[j][1] = xn[j][1]; }
#pragma unroll
      for (int i = 0; i < NT; ++i) wf0[i] = wn[i];
      wrd = wnext;
      if (c == 1) { xwr = xrd; xrd = xnext; }
    }
  }

  constexpr unsigned CS = K1_EX, RS = K1_EX + 6144;
#pragma unroll
  for (int i = 0; i < NT; ++i)
    *(LAS float*)(size_t)(CS + ((((T * 2 + nlo) * 3 + i) * 4 + wm * 2 + h) * 32 + n) * 4) = cs0[i] + cs1[i];
#pragma unroll
  for (int rb = 0; rb < 2; ++rb) {
    auto a = __builtin_amdgcn_permlane32_swap(__float_as_uint(rs[rb]), __float_as_uint(rs[rb]), false, false);
    auto b = __builtin_amdgcn_permlane32_swap(__float_as_uint(rq[rb]), __float_as_uint(rq[rb]), false, false);
    f32x2 val = {__uint_as_float(a[0]) + __uint_as_float(a[1]), __uint_as_float(b[0]) + __uint_as_float(b[1])};
    if (h == 0) *(LAS f32x2*)(size_t)(RS + (((wm * 2 + rb) * 32 + n) * 4 + T * 2 + nlo) * 8) = val;
  }
  wait_lgkm<0>();
  barrier();
  float mean[2], rstd[2];
#pragma unroll
  for (int rb = 0; rb < 2; ++rb) {
    f32x4 a = lds_rdf4(RS + ((wm * 2 + rb) * 32 + n) * 32), b = lds_rdf4(RS + ((wm * 2 + rb) * 32 + n) * 32 + 16);
    float s = (a[0] + a[2]) + (b[0] + b[2]), q = (a[1] + a[3]) + (b[1] + b[3]);
    float m = s * (1.f / 640.f);
    float var = __builtin_fmaxf(__builtin_fmaf(-m, m, q * (1.f / 640.f)), 0.f);
    float sd = bfr(__builtin_sqrtf(bfr(bfr(var) + 1e-5f)));
    mean[rb] = bfr(m);
    rstd[rb] = 1.f / sd;
  }

#pragma unroll
  for (int i = 0; i < NT; ++i) {
    float cs[16];
    {
      const unsigned cb = CS + ((T * 2 + nlo) * 3 + i) * 512 + h * 16;
#pragma unroll
      for (int a = 0; a < 4; ++a) {
        f32x4 t0 = lds_rdf4(cb + a * 32), t1 = lds_rdf4(cb + a * 32 + 128), t2 = lds_rdf4(cb + a * 32 + 256), t3 = lds_rdf4(cb + a * 32 + 384);
#pragma unroll
        for (int e = 0; e < 4; ++e) cs[4 * a + e] = (t0[e] + t1[e]) + (t2[e] + t3[e]);
      }
    }
    bool isq; int unit, sel;
    if (T == 1) { isq = false; unit = odd ? 3 * p + 2 : 3 * p + 1; sel = 2 * nlo + i; }
    else if (odd) { isq = true; int j = 3 * nlo + i; unit = 4 * p + 1 + (j >> 1); sel = j & 1; }
    else { isq = (i == 0); unit = (i == 0) ? 4 * p : 3 * p; sel = (i == 0) ? nlo : 2 * nlo + i - 1; }
    if (isq) {
      if (unit < 30) {
        const int cu0 = 8 * unit + 4 * sel;
        const float sc = cu0 < 80 ? QSCALE : 1.f;
#pragma unroll
        for (int rb = 0; rb < 2; ++rb) {
          const int tb = 4 * mt + 2 * wm + rb;
          const float r = rstd[rb] * sc, m = mean[rb];
          unsigned pkd[8];
#pragma unroll
          for (int e = 0; e < 8; ++e)
            pkd[e] = pk2(__builtin_fmaf(-m, cs[2 * e], acc[i][rb][2 * e]) * r,
                         __builtin_fmaf(-m, cs[2 * e + 1], acc[i][rb][2 * e + 1]) * r);
#pragma unroll
          for (int a = 0; a < 2; ++a) {
            auto s0 = __builtin_amdgcn_permlane32_swap(pkd[4 * a], pkd[4 * a + 2], false, false);
            auto s1 = __builtin_amdgcn_permlane32_swap(pkd[4 * a + 1], pkd[4 * a + 3], false, false);
            u32x4 val = {s0[0], s1[0], s0[1], s1[1]};
            const int cu = cu0 + 2 * a + h;
            const int P = (cu * 205) >> 10, u5 = cu - 5 * P;
            const int rr = P >= 32 ? (n ^ (4 * (u5 & 3))) : n;
            *(u32x4*)(ws + QKV_OFF + (size_t)((P * 64 + tb) * 2560 + u5 * 512 + rr * 16)) = val;
          }
        }
      }
    } else {
#pragma unroll
      for (int rb = 0; rb < 2; ++rb) {
        const int tb = 4 * mt + 2 * wm + rb;
        const float r = rstd[rb], m = mean[rb], rn = -LOG2E * r;
        float hv[8];
#pragma unroll
        for (int e = 0; e < 8; ++e) {
          float g = __builtin_fmaf(-m, cs[e], acc[i][rb][e]) * rn;
          float u = __builtin_fmaf(-m, cs[e + 8], acc[i][rb][e + 8]) * r;
          hv[e] = u * __builtin_amdgcn_rcpf(1.f + __builtin_amdgcn_exp2f(g));
        }
        u32x4 val = {pk2(hv[0], hv[1]), pk2(hv[2], hv[3]), pk2(hv[4], hv[5]), pk2(hv[6], hv[7])};
        const int rowblk = 2 * tb + (n >> 4), ks = 2 * unit + (sel >> 1), kg = 2 * (sel & 1) + h;
        *(u32x4*)(ws + A2_OFF + (size_t)(((rowblk * 68 + ks) * 64 + kg * 16 + (n & 15)) * 16)) = val;
      }
    }
  }
}

__global__ __launch_bounds__(512, 2) void k1_gemm1(const char* __restrict__ x, const char* __restrict__ wqkv,
                                                   const char* __restrict__ wg, const char* __restrict__ wu,
                                                   char* __restrict__ ws) {
  const int b = blockIdx.x, xcd = b & 7, i = b >> 3;
  const int odd = i & 1, mt = i >> 1, p = xcd;
  const int w = __builtin_amdgcn_readfirstlane(threadIdx.x >> 6), lane = threadIdx.x & 63;
  if (w < 4) k1_body<0>(x, wqkv, wg, wu, ws, p, odd, mt, w, lane);
  else k1_body<1>(x, wqkv, wg, wu, ws, p, odd, mt, w, lane);
}

constexpr unsigned K2_V = 24576, K2_ONES = 49152, K2_LDS = 49152 + 64;

DI void retile_job(const char* __restrict__ wo, const char* __restrict__ wd, char* __restrict__ ws, int job, int lane) {
  const int ks = job / 10, G = job - 10 * ks;
  const int kg = lane >> 4, c4 = lane & 15;
  u32x2 r[8];
  if (ks < 48) {
    const char* src = wd + (size_t)(32 * ks + 16 * (kg >> 1) + 4 * (kg & 1)) * 1280 + G * 128 + c4 * 8;
#pragma unroll
    for (int j = 0; j < 8; ++j) r[j] = *(const u32x2*)(src + (8 * (j >> 2) + (j & 3)) * 1280);
  } else {
    const int u = 4 * (ks - 48) + kg, pr = (u * 205) >> 10, i5 = u - 5 * pr;
    const int row0 = i5 < 4 ? 40 * pr + 20 * (i5 >> 1) + 4 * (i5 & 1) : 40 * pr + 16;
    const int step = i5 < 4 ? 8 : 20;
    const char* src = wo + (size_t)row0 * 1280 + G * 128 + c4 * 8;
#pragma unroll
    for (int j = 0; j < 8; ++j) r[j] = *(const u32x2*)(src + (step * (j >> 2) + (j & 3)) * 1280);
  }
  char* dst = ws + B2_OFF + (size_t)((((4 * G + (c4 >> 2)) * 68 + ks) * 64 + kg * 16 + 4 * (c4 & 3)) * 16);
#pragma unroll
  for (int cc = 0; cc < 4; ++cc) {
    u32x4 o;
#pragma unroll
    for (int e = 0; e < 4; ++e) {
      unsigned a = r[2 * e][cc >> 1], b = r[2 * e + 1][cc >> 1];
      o[e] = (cc & 1) ? __builtin_amdgcn_perm(b, a, 0x07060302u) : __builtin_amdgcn_perm(b, a, 0x05040100u);
    }
    *(u32x4*)(dst + cc * 16) = o;
  }
}

__global__ __launch_bounds__(512, 2) void k2_attn(const char* __restrict__ wo, const char* __restrict__ wd,
                                                  char* __restrict__ ws) {
  const int b = blockIdx.x, xcd = b & 7, i = b >> 3;
  const int head = 4 * xcd + (i & 3), QB = i >> 2;
  const int w = __builtin_amdgcn_readfirstlane(threadIdx.x >> 6), lane = threadIdx.x & 63;
  const int pair = head >> 1, e = head & 1;
  const int h = lane >> 5, n = lane & 31;
  const char* qkv = ws + QKV_OFF;

#pragma unroll
  for (int j = 0; j < 6; ++j) {
    const int id = 8 * j + w;
    const int isv = id >= 24, c = isv ? id - 24 : id;
    const int o = c * 1024 + lane * 16, blk = o / 1536, within = o - blk * 1536;
    int tb = 8 * QB - 8 + blk; if (tb < 0) tb = 0;
    const unsigned voff = (unsigned)(((16 + 16 * isv + pair) * 64 + tb) * 2560 + e * 1024 + within);
    dma16(voff, qkv, id * 1024);
  }
  if (threadIdx.x < 4) *(LAS unsigned*)(size_t)(K2_ONES + threadIdx.x * 4) = 0x3f803f80u;
  const int qb = 8 * QB + w;
  const char* qsrc = qkv + (size_t)((pair * 64 + qb) * 2560 + n * 16);
  bf16x8 q1 = *(const bf16x8*)(qsrc + (e ? 3 + h : h) * 512);
  u32x2 q2u = *(const u32x2*)(qsrc + 1024 + 8 * e);
  if (h) { q2u[0] = 0; q2u[1] = 0; }
  const s16x4 q2 = __builtin_bit_cast(s16x4, q2u);

  {
    const int job = b * 8 + w;
    if (job < 680) retile_job(wo, wd, ws, job, lane);
  }

  asm volatile("s_waitcnt vmcnt(0) lgkmcnt(0)" ::: "memory");
  barrier();

  const unsigned ka1 = w * 1536 + (e + h) * 512 + n * 16;
  const unsigned ka2 = w * 1536 + (e ? 8 : 1024) + n * 16;
  const int jmin = QB == 0 ? 8 - w : 0;
  f32x16 s[9];
  const float NEG = -__builtin_inff();
#pragma unroll
  for (int j = 0; j < 9; ++j) {
    if (j >= jmin) {
      bf16x8 k1 = lds_rd128(ka1 + j * 1536);
      s16x4 k2 = *(LAS s16x4*)(size_t)(ka2 + j * 1536);
      f32x16 a = mfma32(k1, q1, (f32x16)(0.f));
      a = __builtin_amdgcn_mfma_f32_32x32x8bf16_1k(k2, q2, a, 0, 0, 0);
      if (j == 0) {
#pragma unroll
        for (int r = 0; r < 16; ++r) a[r] = ((r & 3) + 8 * (r >> 2) + 4 * h > n) ? a[r] : NEG;
      }
      if (j == 8) {
#pragma unroll
        for (int r = 0; r < 16; ++r) a[r] = ((r & 3) + 8 * (r >> 2) + 4 * h <= n) ? a[r] : NEG;
      }
      s[j] = a;
    } else {
      s[j] = (f32x16)(NEG);
    }
  }
  float mx = s[8][0];
#pragma unroll
  for (int j = 0; j < 9; ++j)
#pragma unroll
    for (int r = 0; r < 16; ++r) mx = __builtin_fmaxf(mx, s[j][r]);
  {
    auto t = __builtin_amdgcn_permlane32_swap(__float_as_uint(mx), __float_as_uint(mx), false, false);
    mx = __builtin_fmaxf(__uint_as_float(t[0]), __uint_as_float(t[1]));
  }
  unsigned va0, va1, vstepB, vstepS;
  {
    const int g1 = (lane >> 4) & 1, q = (lane >> 2) & 3, pp = lane & 3;
    const int cc = 5 * e + (g1 ? 4 : pp);
    const int ua = cc >> 1, X = 4 * (ua & 3);
    const unsigned base = K2_V + w * 1536 + (ua - 2 * e) * 512 + (cc & 1) * 8;
    const bool ones = g1 && pp;
    va0 = ones ? K2_ONES : base + (((4 * h + q) ^ X) << 4);
    va1 = ones ? K2_ONES : base + (((8 + 4 * h + q) ^ X) << 4);
    vstepB = ones ? 0 : 1536;
    vstepS = ones ? 0 : 256;
  }
  f32x16 o = (f32x16)(0.f);
#pragma unroll
  for (int j = 0; j < 9; ++j) {
    if (j >= jmin) {
#pragma unroll
      for (int sh = 0; sh < 2; ++sh) {
        u32x4 pu;
#pragma unroll
        for (int r = 0; r < 4; ++r)
          pu[r] = pk2(__builtin_amdgcn_exp2f(s[j][8 * sh + 2 * r] - mx), __builtin_amdgcn_exp2f(s[j][8 * sh + 2 * r + 1] - mx));
        bf16x4 v0 = lds_tr(va0 + j * vstepB + sh * vstepS), v1 = lds_tr(va1 + j * vstepB + sh * vstepS);
        bf16x8 vf = __builtin_shufflevector(v0, v1, 0, 1, 2, 3, 4, 5, 6, 7);
        o = mfma32(vf, __builtin_bit_cast(bf16x8, pu), o);
      }
    }
  }
  const float il = 1.f / o[12];
  const int rowblk = 2 * qb + (n >> 4);
  char* a2 = ws + A2_OFF + (size_t)(rowblk * 68 * 1024 + (n & 15) * 16);
  {
    u32x4 val = {pk2(o[0] * il, o[1] * il), pk2(o[2] * il, o[3] * il), pk2(o[4] * il, o[5] * il), pk2(o[6] * il, o[7] * il)};
    const int u = 5 * pair + 2 * e + h;
    *(u32x4*)(a2 + (48 + (u >> 2)) * 1024 + (u & 3) * 256) = val;
    if (h == 0) {
      u32x2 v2 = {pk2(o[8] * il, o[9] * il), pk2(o[10] * il, o[11] * il)};
      const int uc = 5 * pair + 4;
      *(u32x2*)(a2 + (48 + (uc >> 2)) * 1024 + (uc & 3) * 256 + 8 * e) = v2;
    }
  }
}

constexpr unsigned K3_LDS = 4 * 20480;

__global__ __launch_bounds__(256, 1) void k3_gemm2(const char* __restrict__ x, const char* __restrict__ ws,
                                                   char* __restrict__ out) {
  const int b = blockIdx.x, xcd = b & 7, i = b >> 3;
  const int rg = 4 * xcd + (i >> 3), ct = i & 7;
  const int w = __builtin_amdgcn_readfirstlane(threadIdx.x >> 6), lane = threadIdx.x & 63;
  const char* ap = ws + A2_OFF + (size_t)((4 * rg * 68 + 17 * w) * 1024) + lane * 16;
  const char* bp = ws + B2_OFF + (size_t)((5 * ct * 68 + 17 * w) * 1024) + lane * 16;
  constexpr int PF = 4;
  bf16x8 af[PF][4], bf[PF][5];
  f32x4 acc[4][5];
#pragma unroll
  for (int r = 0; r < 4; ++r)
#pragma unroll
    for (int c = 0; c < 5; ++c) acc[r][c] = (f32x4)(0.f);
  auto load = [&](int ks, int slot) {
#pragma unroll
    for (int r = 0; r < 4; ++r) af[slot][r] = *(const bf16x8*)(ap + (r * 68 + ks) * 1024);
#pragma unroll
    for (int c = 0; c < 5; ++c) bf[slot][c] = *(const bf16x8*)(bp + (c * 68 + ks) * 1024);
  };
#pragma unroll
  for (int k = 0; k < PF - 1; ++k) load(k, k);
#pragma unroll
  for (int k = 0; k < 17; ++k) {
    if (k + PF - 1 < 17) load(k + PF - 1, (k + PF - 1) % PF);
    __builtin_amdgcn_sched_barrier(0);
#pragma unroll
    for (int r = 0; r < 4; ++r)
#pragma unroll
      for (int c = 0; c < 5; ++c)
        acc[r][c] = __builtin_amdgcn_mfma_f32_16x16x32_bf16(bf[k % PF][c], af[k % PF][r], acc[r][c], 0, 0, 0);
    __builtin_amdgcn_sched_barrier(0);
  }
  const int tok = lane & 15, g = lane >> 4;
  const size_t ooff = (size_t)(64 * rg + 16 * w + tok) * 1280 + ct * 160 + g * 8;
  u32x2 xr[5];
#pragma unroll
  for (int c = 0; c < 5; ++c) xr[c] = *(const u32x2*)(x + ooff + c * 32);
#pragma unroll
  for (int r = 0; r < 4; ++r)
#pragma unroll
    for (int c = 0; c < 5; ++c) *(LAS f32x4*)(size_t)(w * 20480 + ((r * 5 + c) * 64 + lane) * 16) = acc[r][c];
  __syncthreads();
#pragma unroll
  for (int c = 0; c < 5; ++c) {
    const unsigned a = ((w * 5 + c) * 64 + lane) * 16;
    f32x4 t = (lds_rdf4(a) + lds_rdf4(a + 20480)) + (lds_rdf4(a + 40960) + lds_rdf4(a + 61440));
    u32x2 val = {pk2(t[0] + lo_f(xr[c][0]), t[1] + hi_f(xr[c][0])), pk2(t[2] + lo_f(xr[c][1]), t[3] + hi_f(xr[c][1]))};
    *(u32x2*)(out + ooff + c * 32) = val;
  }
}

extern "C" void kernel_launch(void* const* d_in, const int* in_sizes, int n_in, void* d_out, int out_size,
                              void* d_ws, size_t ws_size, hipStream_t stream) {
  const char* x = (const char*)d_in[0];
  const char* wqkv = (const char*)d_in[1];
  const char* wo = (const char*)d_in[2];
  const char* wg = (const char*)d_in[4];
  const char* wu = (const char*)d_in[5];
  const char* wd = (const char*)d_in[6];
  char* ws = (char*)d_ws;
  (void)hipFuncSetAttribute((const void*)k1_gemm1, hipFuncAttributeMaxDynamicSharedMemorySize, K1_LDS);
  (void)hipFuncSetAttribute((const void*)k3_gemm2, hipFuncAttributeMaxDynamicSharedMemorySize, K3_LDS);
  hipLaunchKernelGGL(k1_gemm1, dim3(256), dim3(512), K1_LDS, stream, x, wqkv, wg, wu, ws);
  hipLaunchKernelGGL(k2_attn, dim3(256), dim3(512), K2_LDS, stream, wo, wd, ws);
  hipLaunchKernelGGL(k3_gemm2, dim3(256), dim3(256), K3_LDS, stream, x, (const char*)ws, (char*)d_out);
}
```

```cpp
#include <hip/hip_runtime.h>
#include <hip/hip_bf16.h>
#include <stdint.h>
#include <utility>

typedef __attribute__((ext_vector_type(2))) __bf16 bf16x2;
typedef __attribute__((ext_vector_type(4))) __bf16 bf16x4;
typedef __attribute__((ext_vector_type(8))) __bf16 bf16x8;
typedef __attribute__((ext_vector_type(4))) short s16x4;
typedef __attribute__((ext_vector_type(2))) float f32x2;
typedef __attribute__((ext_vector_type(4))) float f32x4;
typedef __attribute__((ext_vector_type(16))) float f32x16;
typedef __attribute__((ext_vector_type(2))) unsigned u32x2;
typedef __attribute__((ext_vector_type(4))) unsigned u32x4;
#define LAS __attribute__((address_space(3)))
#define DI __device__ __forceinline__
#define SB() __builtin_amdgcn_sched_barrier(0)
#define IC(v) std::integral_constant<int, (v)>{}

constexpr unsigned QKV_OFF = 0;
constexpr unsigned A2_OFF = 7864320;
constexpr unsigned B2_OFF = 16777216;
constexpr unsigned CT_OFF = B2_OFF + 2785280;
constexpr unsigned F1_OFF = CT_OFF, F2_OFF = CT_OFF + 1024, F3_OFF = CT_OFF + 2048, GEN_OFF = CT_OFF + 3072;
constexpr unsigned GR_OFF = CT_OFF + 4096;
constexpr unsigned WS_END = GR_OFF + 16384;
constexpr unsigned OOB = 0x80000000u;

#define STAMP_DECL
#define STAMP(S)
#define STAMP_FLUSH(K)

constexpr float LOG2E = 1.4426950408889634f;
constexpr float QSCALE = 0.22360679774997896f * LOG2E;

template <class F, int... I> DI void static_for_impl(F&& f, std::integer_sequence<int, I...>) { (f(std::integral_constant<int, I>{}), ...); }
template <int N, class F> DI void static_for(F&& f) { static_for_impl(f, std::make_integer_sequence<int, N>{}); }

DI void dma16(unsigned voff, const void* sbase, unsigned lds) {
  asm volatile("s_mov_b32 m0, %2\n\ts_nop 0\n\tglobal_load_lds_dwordx4 %0, %1"
               :: "v"(voff), "s"(sbase), "s"(lds) : "memory");
}
template <int N> DI void wait_vm() { asm volatile("s_waitcnt vmcnt(%0)" :: "i"(N) : "memory"); }
template <int N, int M> DI void wait_vm_lgkm() { asm volatile("s_waitcnt vmcnt(%0) lgkmcnt(%1)" :: "i"(N), "i"(M) : "memory"); }
template <int M> DI void wait_lgkm() { asm volatile("s_waitcnt lgkmcnt(%0)" :: "i"(M) : "memory"); }
DI void barrier() { __builtin_amdgcn_s_barrier(); asm volatile("" ::: "memory"); }

DI bf16x8 lds_rd128(unsigned a) { return *(LAS bf16x8*)(size_t)a; }
DI f32x4 lds_rdf4(unsigned a) { return *(LAS f32x4*)(size_t)a; }
DI bf16x4 lds_tr(unsigned a) { return __builtin_amdgcn_ds_read_tr16_b64_v4bf16((LAS bf16x4*)(size_t)a); }
DI bf16x8 lds_trfrag(unsigned a, unsigned step) {
  bf16x4 lo = lds_tr(a), hi = lds_tr(a + step);
  return __builtin_shufflevector(lo, hi, 0, 1, 2, 3, 4, 5, 6, 7);
}
DI unsigned pk2(float a, float b) {
  f32x2 v = {a, b};
  return __builtin_bit_cast(unsigned, __builtin_convertvector(v, bf16x2));
}
DI float bfr(float a) { return (float)(__bf16)a; }
DI float lo_f(unsigned u) { return __uint_as_float(u << 16); }
DI float hi_f(unsigned u) { return __uint_as_float(u & 0xffff0000u); }
DI f32x16 mfma32(bf16x8 a, bf16x8 b, f32x16 c) { return __builtin_amdgcn_mfma_f32_32x32x16_bf16(a, b, c, 0, 0, 0); }
DI f32x4 mfma16(bf16x8 a, bf16x8 b, f32x4 c) { return __builtin_amdgcn_mfma_f32_16x16x32_bf16(a, b, c, 0, 0, 0); }
DI f32x16 mfma8(s16x4 a, s16x4 b, f32x16 c) { return __builtin_amdgcn_mfma_f32_32x32x8bf16_1k(a, b, c, 0, 0, 0); }
typedef __amdgpu_buffer_rsrc_t rsrc_t;
DI rsrc_t mkrsrc(const void* p, unsigned n) { return __builtin_amdgcn_make_buffer_rsrc((void*)p, 0, n, 0x00020000); }
DI void st128(rsrc_t r, unsigned vo, unsigned so, u32x4 v) {
  __builtin_amdgcn_raw_buffer_store_b128(v, r, vo, so, 16);
  asm volatile("s_nop 1" :: "v"(v) : "memory");
}
DI void st64(rsrc_t r, unsigned vo, unsigned so, u32x2 v) { __builtin_amdgcn_raw_buffer_store_b64(v, r, vo, so, 16); }
DI void st32(rsrc_t r, unsigned vo, unsigned so, unsigned v) { __builtin_amdgcn_raw_buffer_store_b32(v, r, vo, so, 16); }
DI u32x4 ld128(rsrc_t r, unsigned vo, unsigned so) { return __builtin_amdgcn_raw_buffer_load_b128(r, vo, so, 16); }
DI u32x2 ld64(rsrc_t r, unsigned vo, unsigned so) { return __builtin_amdgcn_raw_buffer_load_b64(r, vo, so, 16); }
DI unsigned ld32(rsrc_t r, unsigned vo, unsigned so) { return __builtin_amdgcn_raw_buffer_load_b32(r, vo, so, 16); }
DI u32x4 mkdesc(const void* p, unsigned n) {
  const unsigned long long a = (unsigned long long)p;
  return (u32x4){(unsigned)a, (unsigned)(a >> 32) & 0xffffu, n, 0x00020000u};
}
template <int CTRL> DI float dpp_add(float v) {
  return v + __uint_as_float(__builtin_amdgcn_update_dpp(0, __float_as_uint(v), CTRL, 0xf, 0xf, true));
}
DI float wave_sum(float v) {
  v = dpp_add<0x111>(v); v = dpp_add<0x112>(v); v = dpp_add<0x114>(v); v = dpp_add<0x118>(v);
  const float a = __uint_as_float(__builtin_amdgcn_readlane(__float_as_uint(v), 15));
  const float b = __uint_as_float(__builtin_amdgcn_readlane(__float_as_uint(v), 31));
  const float c = __uint_as_float(__builtin_amdgcn_readlane(__float_as_uint(v), 47));
  const float d = __uint_as_float(__builtin_amdgcn_readlane(__float_as_uint(v), 63));
  return (a + b) + (c + d);
}
DI int fresh_lane() {
  int l; asm volatile("v_mbcnt_lo_u32_b32 %0, -1, 0\n\tv_mbcnt_hi_u32_b32 %0, -1, %0" : "=v"(l)); return l;
}
#define CSON true
constexpr unsigned SPIN_CAP = 1u << 16;

constexpr unsigned XOFF = 98304, XSLOT = 16384, EXO = 32768, EX2 = EXO + 6144;
constexpr unsigned xsl(int s) { return XOFF + (unsigned)(s & 3) * XSLOT; }
constexpr unsigned wsl1(int s) { return (unsigned)(s & 3) * 24576u; }
constexpr unsigned wsl2(int u) {
  return u == 0 ? 49152u : u == 1 ? 65536u : u == 2 ? 81920u : u == 3 ? 0u : u == 4 ? 16384u :
         u == 5 ? 49152u : u == 6 ? xsl(6) : u == 7 ? 65536u : u == 8 ? xsl(7) : 81920u;
}
constexpr unsigned KS = 1552, VS = 1808, KO = 81920 + 16384, VO = KO + 16 * KS;
constexpr unsigned CNT2 = 163840 - 64;
constexpr unsigned RP = 0, RQ = 81920;
static_assert(VO + 16 * VS <= CNT2, "lds");

#define RT_LD 22
#define RT_ST 33
#define GR_LD 34
#define FL1K 55
#define PL1K 64
#define KVK 70
struct Ops { short id[256]; short k[256]; short dma[256]; int n; };
constexpr int dma_at(int K, int slot) {
  const int G = (K - 3) >> 2, rel = (K - 3) & 3;
  if (K < 3) return -1;
  if (G <= 5) {
    const int s = G + 4;
    if (rel == 0) return slot == 0 ? 100 + 5 * s : 100 + 5 * s + 2;
    if (slot) return -1;
    return rel == 1 ? 100 + 5 * s + 1 : 100 + 5 * s + 1 + rel;
  }
  if (slot) return -1;
  int u0 = -1, cnt = 0;
  if (G == 6) { u0 = 0; cnt = 2; }
  if (G == 7) { u0 = 1; cnt = 4; }
  if (G == 8) { u0 = 3; cnt = 2; }
  if (G == 9) { u0 = 4; cnt = 2; }
  if (G == 10) { u0 = 5; cnt = 4; }
  if (G == 11) { u0 = 7; cnt = 4; }
  if (G == 12) { u0 = 9; cnt = 2; }
  return rel < cnt ? 200 + 2 * u0 + rel : -1;
}
constexpr int E1K = 43;
constexpr Ops make_ops(bool odd) {
  Ops o{}; int n = 0;
  auto add = [&](int id, int k, int d) { o.id[n] = (short)id; o.k[n] = (short)k; o.dma[n] = (short)d; ++n; };
  add(1, -3, 0); add(2, -3, 0); add(3, -3, 0);
  for (int i = 0; i < 10; ++i) add(100 + i, -3, 1);
  add(4, -2, 0);
  for (int i = 10; i < 20; ++i) add(100 + i, -1, 1);
  for (int K = 0; K < 80; ++K) {
    for (int s = 0; s < 2; ++s) if (dma_at(K, s) >= 0) add(dma_at(K, s), K, 1);
    if (K == RT_LD) for (int i = 0; i < 8; ++i) add(300, K, 0);
    if (K == RT_ST) for (int i = 0; i < 4; ++i) add(310, K, 0);
    if (K == GR_LD) add(320, K, 0);
    if (K >= E1K && K < E1K + 2) add(330, K, 0);
    if (odd && K >= E1K + 2 && K < E1K + 6) add(330, K, 0);
    if (!odd && (K == E1K + 5 || K == E1K + 9)) add(331, K, 0);
    if (K == PL1K) add(340, K, 0);
    if (K == KVK) for (int i = 0; i < 8; ++i) add(350, K, 0);
    if (K > KVK) add(350, K, 0);
  }
  o.n = n; return o;
}
constexpr Ops OPA = make_ops(true), OPB = make_ops(false);
constexpr bool stage_has(int id, int S) {
  return (id >= 100 && id < 200) ? (id - 100) / 5 <= S : (id >= 200 && id < 300) ? (id - 200) / 2 + 10 <= S : false;
}
constexpr int wait_stage(const Ops& o, int K, int S) {
  int issued = 0, last = -1;
  for (int i = 0; i < o.n; ++i) { if (o.k[i] < K) issued = i + 1; if (stage_has(o.id[i], S)) last = i; }
  const int w = issued - 1 - last; return w < 0 ? 0 : (w > 63 ? 63 : w);
}
constexpr int wait_mid(const Ops& o, int K, int id) {
  int issued = 0, last = -1;
  for (int i = 0; i < o.n; ++i) { if (o.k[i] < K || (o.k[i] == K && o.dma[i])) issued = i + 1; if (o.id[i] == id) last = i; }
  const int w = issued - 1 - last; return w < 0 ? 0 : (w > 63 ? 63 : w);
}

__global__ __launch_bounds__(512, 2) void fused(const char* __restrict__ x, const char* __restrict__ wqkv,
                                                const char* __restrict__ wo, const char* __restrict__ wg,
                                                const char* __restrict__ wu, const char* __restrict__ wd,
                                                char* __restrict__ ws, char* __restrict__ out) {
  const int b = blockIdx.x, xcd = b & 7, ii = b >> 3;
  const int odd = ii & 1, mt = ii >> 1, p = xcd, g = 2 * p + odd;
  const int w = __builtin_amdgcn_readfirstlane(threadIdx.x >> 6);
  const int rgp = w & 3, nlo = w >> 2;
  int lane = threadIdx.x & 63, h = lane >> 5, n = lane & 31;
  unsigned l16 = lane * 16;
#define REFRESH() do { lane = fresh_lane(); h = lane >> 5; n = lane & 31; l16 = lane * 16; } while (0)
  const int r0 = (mt + xcd) % 10;
  const rsrc_t rws = mkrsrc(ws, WS_END);
  STAMP_DECL; STAMP(0);

  u32x4 sx4; unsigned sx1, gen;
  const int srow = 128 * mt + 8 * g + w;
  {
    const char* rp = x + (size_t)srow * 1280;
    asm volatile("global_load_dwordx4 %0, %1, %2" : "=v"(sx4) : "v"(lane * 16), "s"(rp) : "memory");
    asm volatile("global_load_dword %0, %1, %2 offset:1024" : "=v"(sx1) : "v"(lane * 4), "s"(rp) : "memory");
    asm volatile("global_load_dword %0, %1, %2 sc1" : "=v"(gen) : "v"(0), "s"(ws + GEN_OFF) : "memory");
  }
  const char* wb[3]; unsigned wstep[3], wv[3];
  const char* gb; const char* ub; unsigned gv, uv;
  {
    const int r = lane >> 3, u = lane & 7, sw = ((r >> 1) & 1) << 2;
    const unsigned vq = r * 3840 + ((u ^ sw) << 4);
    gv = r * 3072 + ((u ^ sw) << 4);
    uv = r * 3072 + ((u ^ sw ^ 2) << 4);
#pragma unroll
    for (int j = 0; j < 3; ++j) {
      const bool isq = odd || j == 0;
      int unit = odd ? 4 * p + 1 + j : (j == 0 ? 4 * p : 3 * p);
      if (isq && unit > 29) unit = 29;
      const unsigned stride = isq ? 3840u : 3072u;
      wb[j] = (isq ? wqkv : (j == 1 ? wg : wu)) + unit * 128 + (size_t)(8 * w) * stride;
      wstep[j] = 64 * stride;
      wv[j] = isq ? vq : (j == 1 ? gv : uv);
    }
    const int u2 = 3 * p + 1 + odd;
    gb = wg + u2 * 128 + (size_t)(8 * w) * 3072;
    ub = wu + u2 * 128 + (size_t)(8 * w) * 3072;
  }
  const char* xb = x + (size_t)(128 * mt + 16 * w) * 1280;
  unsigned xv0, xv1;
  { const int r = lane >> 3, u = lane & 7; xv0 = r * 1280 + ((u ^ (r >> 1)) << 4); xv1 = (r + 8) * 1280 + ((u ^ (4 + (r >> 1))) << 4); }
  auto kst = [&](int pos) { int k = r0 + pos; return k >= 10 ? k - 10 : k; };
  auto issue = [&](auto IT) {
    constexpr int it = IT;
    if constexpr (it >= 200) {
      constexpr int u = (it - 200) >> 1, j = it & 1;
      dma16(j ? uv : gv, (j ? ub : gb) + (size_t)kst((u + 6) % 10) * (64 * 3072), wsl2(u) + j * 8192 + w * 1024);
    } else if constexpr (it >= 100) {
      constexpr int s = (it - 100) / 5, i = (it - 100) % 5;
      if constexpr (i < 2) dma16(i ? xv1 : xv0, xb + kst(s) * 128, xsl(s) + w * 2048 + i * 1024);
      else dma16(wv[i - 2], wb[i - 2] + (size_t)kst(s) * wstep[i - 2], wsl1(s) + (i - 2) * 8192 + w * 1024);
    }
  };
  static_for<10>([&](auto I) { issue(IC(100 + I)); });
  STAMP(1);

  unsigned wa1[3], wa2[2], xa[4];
  {
    const int cb = (lane >> 4) & 1, q = (lane >> 2) & 3, pp = lane & 3;
    const unsigned rowoff = (8 * h + q) * 128 + (pp & 1) * 8;
    const int sw = (q >> 1) << 2;
#pragma unroll
    for (int i = 0; i < 3; ++i) {
      const int j = 3 * nlo + i;
      const int piece = odd ? (j >> 1) : 0, sel = odd ? (j & 1) : nlo, t = 2 * nlo + i - 1;
      const unsigned aq = piece * 8192 + (((4 * sel + 2 * cb + (pp >> 1)) ^ sw) << 4);
      const unsigned ag = (1 + cb) * 8192 + (((2 * t + (pp >> 1)) ^ sw ^ (cb << 1)) << 4);
      wa1[i] = rowoff + ((odd || i == 0) ? aq : ag);
    }
#pragma unroll
    for (int i = 0; i < 2; ++i) wa2[i] = rowoff + cb * 8192 + (((2 * (2 * nlo + i) + (pp >> 1)) ^ sw ^ (cb << 1)) << 4);
    const int row = 32 * rgp + n, sx = (row >> 1) & 7;
#pragma unroll
    for (int j = 0; j < 4; ++j) xa[j] = row * 128 + (((2 * j + h) ^ sx) << 4);
  }
  bf16x8 selv;
  {
    const int i = lane & 15, kg = lane >> 4;
    const unsigned o1 = ((i & 3) == 0 && ((i >> 2) & 1) == (kg & 1)) ? 0x3f803f80u : 0u;
    u32x4 t = {o1, o1, o1, o1};
    selv = __builtin_bit_cast(bf16x8, t);
  }

  unsigned tag;
  {
    asm volatile("s_waitcnt vmcnt(10)" : "+v"(sx4), "+v"(sx1), "+v"(gen) :: "memory");
    tag = __builtin_amdgcn_readfirstlane(gen) * 0x9E3779B1u + 0x7F4A7C15u;
    float v[10];
#pragma unroll
    for (int i = 0; i < 4; ++i) { v[2 * i] = lo_f(sx4[i]); v[2 * i + 1] = hi_f(sx4[i]); }
    v[8] = lo_f(sx1); v[9] = hi_f(sx1);
    float s = 0.f;
#pragma unroll
    for (int i = 0; i < 10; ++i) s += v[i];
    const float m = wave_sum(s) * (1.f / 640.f);
    float q = 0.f;
#pragma unroll
    for (int i = 0; i < 10; ++i) { const float d = v[i] - m; q = __builtin_fmaf(d, d, q); }
    const float var = wave_sum(q) * (1.f / 640.f);
    const float sd = __builtin_sqrtf(bfr(bfr(var) + 1e-5f));
    u32x2 gr = {pk2(m, sd), tag};
    st64(rws, lane == 0 ? 0u : OOB, GR_OFF + srow * 8, gr);
  }
  SB();
  wait_vm<6>();
  barrier();
  STAMP(2);
  static_for<10>([&](auto I) { issue(IC(110 + I)); });

  f32x16 acc1[3], acc2[2];
  f32x4 cs1[3], cs2[2];
#pragma unroll
  for (int i = 0; i < 3; ++i) { acc1[i] = (f32x16)(0.f); cs1[i] = (f32x4)(0.f); }
#pragma unroll
  for (int i = 0; i < 2; ++i) { acc2[i] = (f32x16)(0.f); cs2[i] = (f32x4)(0.f); }
  bf16x8 xr[56];
  bf16x8 wf[2][3];
  u32x2 rt[8];
  u32x2 grn;
  bf16x8 acs[3], negm;
  float rstd, sdv;
  float hv[8];
  u32x4 kv[6], pol;
  bf16x8 q1; u32x2 q2u;
  u32x4 bq[9][5], aq[9][4];

  const int job = b + 256 * w, jok = (w < 3 && job < 680);
  const int jks = job / 10, jG = job - 10 * jks;

  const int head = 4 * xcd + (ii & 3), QB = ii >> 2, pair = head >> 1, e = head & 1;
  const int rg = 4 * xcd + (ii >> 3), ct = ii & 7;
  const int ks0 = w < 4 ? 4 * w : 8 * w - 16;
  const unsigned bso = B2_OFF + (unsigned)(5 * ct * 68) * 1024;
  const unsigned aso = A2_OFF + (unsigned)(4 * rg * 68) * 1024;
  auto ksof = [&](int s) { return (w < 4 && s >= 4) ? 44 + 5 * w + s : ks0 + s; };
  auto ldB = [&](int s, int c) { bq[s][c] = ld128(rws, l16, bso + (c * 68 + ksof(s)) * 1024); };
  auto ldA = [&](int s, int r) { aq[s][r] = ld128(rws, l16, aso + (r * 68 + ksof(s)) * 1024); };

  auto rd = [&](auto KK) {
    constexpr int K = KK;
    if constexpr (K < 40) {
      constexpr int s = K >> 2, j = K & 3;
      xr[K] = lds_rd128(xa[j] + xsl(s));
#pragma unroll
      for (int i = 0; i < 3; ++i) wf[K & 1][i] = lds_trfrag(wa1[i] + wsl1(s) + j * 2048, 512);
    } else if constexpr (K < 80) {
      constexpr int u = (K - 40) >> 2, j = K & 3;
      if constexpr (u < 4) xr[K] = lds_rd128(xa[j] + xsl(6 + u));
#pragma unroll
      for (int i = 0; i < 2; ++i) wf[K & 1][i] = lds_trfrag(wa2[i] + wsl2(u) + j * 2048, 512);
    }
  };
  auto cs_write = [&](f32x4 c, unsigned base) {
    const unsigned u0 = __float_as_uint(c[0]);
    const float r1 = c[0] - __uint_as_float(u0 & 0xffff0000u);
    const unsigned u1 = __float_as_uint(r1);
    const float r2 = r1 - __uint_as_float(u1 & 0xffff0000u);
    u32x2 v = {__builtin_amdgcn_perm(u1, u0, 0x07060302u), __float_as_uint(r2) >> 16};
    *(LAS u32x2*)(size_t)(base + ((rgp >> 1) * 32 + n) * 16 + (rgp & 1) * 8) = v;
  };
  auto qkv_half = [&](const f32x16& a16, int cu0, int a, bool valid) {
    const float r = rstd * (cu0 < 80 ? QSCALE : 1.f);
    unsigned pk[4];
#pragma unroll
    for (int i = 0; i < 4; ++i) pk[i] = pk2(a16[8 * a + 2 * i] * r, a16[8 * a + 2 * i + 1] * r);
    auto s0 = __builtin_amdgcn_permlane32_swap(pk[0], pk[2], false, false);
    auto s1 = __builtin_amdgcn_permlane32_swap(pk[1], pk[3], false, false);
    u32x4 val = {s0[0], s1[0], s0[1], s1[1]};
    const int cu = cu0 + 2 * a + h;
    const int P = (cu * 205) >> 10, u5 = cu - 5 * P;
    const int rr = P >= 32 ? (n ^ (4 * (u5 & 3))) : n;
    const unsigned vo = P * (64 * 2560) + u5 * 512 + rr * 16;
    st128(rws, valid ? vo : OOB, QKV_OFF + (4 * mt + rgp) * 2560, val);
  };
  auto gu_pair = [&](const f32x16& a16, int q) {
    const float rn = -LOG2E * rstd;
#pragma unroll
    for (int i = 2 * q; i < 2 * q + 2; ++i) {
      const float ex = __builtin_amdgcn_exp2f(a16[i] * rn);
      hv[i] = a16[i + 8] * __builtin_amdgcn_rcpf(__builtin_fmaf(ex, sdv, sdv));
    }
  };
  auto gu_store = [&](int unit, int t) {
    u32x4 val = {pk2(hv[0], hv[1]), pk2(hv[2], hv[3]), pk2(hv[4], hv[5]), pk2(hv[6], hv[7])};
    const unsigned vo = (n >> 4) * (68 * 1024) + h * 256 + (n & 15) * 16;
    st128(rws, vo, A2_OFF + ((2 * (4 * mt + rgp) * 68 + 2 * unit + (t >> 1)) * 64 + 2 * (t & 1) * 16) * 16, val);
  };

  rd(IC(0));
  static_for<80>([&](auto KK) {
    constexpr int K = KK, S = K >> 2, j = K & 3;
    if constexpr (j == 3 && S < 19) {
      constexpr int na = K == FL1K ? 0 : wait_stage(OPA, K, S + 1), nb = K == FL1K ? 0 : wait_stage(OPB, K, S + 1);
      if constexpr (na == nb) wait_vm_lgkm<na, 0>();
      else { if (odd) wait_vm_lgkm<na, 0>(); else wait_vm_lgkm<nb, 0>(); }
      barrier();
      if constexpr (K == FL1K) { if (w == 0) st32(rws, fresh_lane() == 0 ? 0u : OOB, F1_OFF + (mt * 16 + g) * 4, tag); }
    }
    if constexpr (K == 41) { wait_lgkm<0>(); barrier(); }
    rd(IC(K + 1));
    if constexpr (dma_at(K, 0) >= 0) issue(IC(dma_at(K, 0)));
    if constexpr (dma_at(K, 1) >= 0) issue(IC(dma_at(K, 1)));
    if constexpr (K == 41) {
#pragma unroll
      for (int i = 0; i < 3; ++i) acs[i] = lds_rd128(EXO + (3 * nlo + i) * 1024 + l16);
    }
    if constexpr (K < 40) {
#pragma unroll
      for (int i = 0; i < 3; ++i) acc1[i] = mfma32(wf[K & 1][i], xr[K], acc1[i]);
      __builtin_amdgcn_sched_group_barrier(0x8, 1, 0); __builtin_amdgcn_sched_group_barrier(0x100, 3, 0);
      __builtin_amdgcn_sched_group_barrier(0x8, 1, 0); __builtin_amdgcn_sched_group_barrier(0x100, 2, 0);
      __builtin_amdgcn_sched_group_barrier(0x8, 1, 0); __builtin_amdgcn_sched_group_barrier(0x100, 2, 0);
      SB();
      if (CSON && rgp == j) {
#pragma unroll
        for (int i = 0; i < 3; ++i) cs1[i] = mfma16(selv, wf[K & 1][i], cs1[i]);
      }
    } else {
      constexpr int u = (K - 40) >> 2;
#pragma unroll
      for (int i = 0; i < 2; ++i) acc2[i] = mfma32(wf[K & 1][i], xr[u < 4 ? K : K - 56], acc2[i]);
      __builtin_amdgcn_sched_group_barrier(0x8, 1, 0); __builtin_amdgcn_sched_group_barrier(0x100, 3, 0);
      __builtin_amdgcn_sched_group_barrier(0x8, 1, 0); __builtin_amdgcn_sched_group_barrier(0x100, 5, 0);
      SB();
      if (CSON && rgp == j) {
#pragma unroll
        for (int i = 0; i < 2; ++i) cs2[i] = mfma16(selv, wf[K & 1][i], cs2[i]);
      }
    }
    SB();
    if constexpr (K == RT_LD || K == RT_ST || K == GR_LD || K == 40 || K == PL1K) REFRESH();
    if constexpr (K == RT_LD) {
      const int kg = lane >> 4, c4 = lane & 15;
      const int u = 4 * (jks - 48) + kg, pr = (u * 205) >> 10, i5 = u - 5 * pr;
      const int row0o = i5 < 4 ? 40 * pr + 20 * (i5 >> 1) + 4 * (i5 & 1) : 40 * pr + 16;
      const int row0 = jks < 48 ? 32 * jks + 16 * (kg >> 1) + 4 * (kg & 1) : row0o;
      const int step = (jks < 48 || i5 < 4) ? 8 : 20;
      const unsigned v0 = row0 * 1280 + jG * 128 + c4 * 8, v1 = v0 + step * 1280;
      const u32x4 rs = mkdesc(jks < 48 ? wd : wo, jok ? (jks < 48 ? 1536 * 1280 : 640 * 1280) : 0);
#pragma unroll
      for (int i = 0; i < 8; ++i)
        asm volatile("buffer_load_dwordx2 %0, %1, %2, 0 offen offset:%3" : "=v"(rt[i]) : "v"(i < 4 ? v0 : v1), "s"(rs), "i"((i & 3) * 1280) : "memory");
    }
    if constexpr (K == RT_ST) {
      static_assert(wait_mid(OPA, K, 300) == wait_mid(OPB, K, 300), "");
      asm volatile("s_waitcnt vmcnt(%8)" : "+v"(rt[0]), "+v"(rt[1]), "+v"(rt[2]), "+v"(rt[3]), "+v"(rt[4]), "+v"(rt[5]), "+v"(rt[6]), "+v"(rt[7])
                   : "i"(wait_mid(OPA, K, 300)) : "memory");
      const int kg = lane >> 4, c4 = lane & 15;
      const unsigned vo = jok ? ((c4 >> 2) * 68 * 64 + kg * 16 + 4 * (c4 & 3)) * 16 : OOB;
#pragma unroll
      for (int cc = 0; cc < 4; ++cc) {
        u32x4 o;
#pragma unroll
        for (int q = 0; q < 4; ++q) {
          const unsigned a = rt[2 * q][cc >> 1], bb = rt[2 * q + 1][cc >> 1];
          o[q] = (cc & 1) ? __builtin_amdgcn_perm(bb, a, 0x07060302u) : __builtin_amdgcn_perm(bb, a, 0x05040100u);
        }
        st128(rws, vo + cc * 16, B2_OFF + (4 * jG * 68 + jks) * 1024, o);
      }
    }
    if constexpr (K == GR_LD) {
      asm volatile("buffer_load_dwordx2 %0, %1, %2, %3 offen sc1" : "=v"(grn) : "v"(n * 8), "s"(mkdesc(ws, WS_END)), "s"(GR_OFF + (128 * mt + 32 * rgp) * 8) : "memory");
    }
    if constexpr (K == 40) {
      STAMP(3);
#pragma unroll
      for (int i = 0; i < 3; ++i) cs_write(cs1[i], EXO + (3 * nlo + i) * 1024);
      static_assert(wait_mid(OPA, K, 320) == wait_mid(OPB, K, 320), "");
      asm volatile("s_waitcnt vmcnt(%1)" : "+v"(grn) : "i"(wait_mid(OPA, K, 320)) : "memory");
      if (!__all(grn[1] == tag)) {
        for (unsigned sp = 0; sp < SPIN_CAP; ++sp) {
          __builtin_amdgcn_s_sleep(1);
          grn = ld64(rws, n * 8, GR_OFF + (128 * mt + 32 * rgp) * 8);
          if (__all(grn[1] == tag)) break;
        }
      }
      const unsigned nm = ((grn[0] & 0xffffu) ^ 0x8000u) * 0x10001u;
      u32x4 t = {nm, nm, nm, nm};
      negm = __builtin_bit_cast(bf16x8, t);
      sdv = hi_f(grn[0]);
      rstd = 1.f / sdv;
    }
    if constexpr (K == 42) {
#pragma unroll
      for (int i = 0; i < 3; ++i) acc1[i] = mfma32(acs[i], negm, acc1[i]);
    }
    if constexpr (K >= E1K && K < E1K + 2) {
      const int j0 = 3 * nlo;
      const int unit = odd ? 4 * p + 1 + (j0 >> 1) : 4 * p, sel = odd ? (j0 & 1) : nlo;
      qkv_half(acc1[0], 8 * unit + 4 * sel, K - E1K, unit < 30);
    }
    if constexpr (K >= E1K + 2 && K < E1K + 10) {
      constexpr int z = K - E1K - 2;
      if (odd) {
        if constexpr (z < 4) {
          const int jj = 3 * nlo + 1 + (z >> 1), unit = 4 * p + 1 + (jj >> 1);
          qkv_half(acc1[1 + (z >> 1)], 8 * unit + 4 * (jj & 1), z & 1, unit < 30);
        }
      } else {
        gu_pair(acc1[1 + (z >> 2)], z & 3);
        if constexpr ((z & 3) == 3) gu_store(3 * p, 2 * nlo + (z >> 2));
      }
    }
    if constexpr (K == PL1K) pol = ld128(rws, l16, F1_OFF);
    if constexpr (K == KVK) {
      STAMP(4);
      if (!__all(pol[0] == tag && pol[1] == tag && pol[2] == tag && pol[3] == tag)) {
        for (unsigned sp = 0; sp < SPIN_CAP; ++sp) {
          __builtin_amdgcn_s_sleep(1);
          pol = ld128(rws, l16, F1_OFF);
          if (__all(pol[0] == tag && pol[1] == tag && pol[2] == tag && pol[3] == tag)) break;
        }
      }
      STAMP(5);
      SB();
#pragma unroll
      for (int i = 0; i < 6; ++i) {
        const int c = (8 * i + w) - (i >= 3 ? 24 : 0);
        const int U0 = 2 * c, U1 = 2 * c + 1, b0 = (U0 * 43) >> 7, b1 = (U1 * 43) >> 7;
        int t0 = 8 * QB - 8 + b0, t1 = 8 * QB - 8 + b1;
        t0 = t0 < 0 ? 0 : t0; t1 = t1 < 0 ? 0 : t1;
        const unsigned o0 = t0 * 2560 + (U0 - 3 * b0) * 512, o1 = t1 * 2560 + (U1 - 3 * b1) * 512;
        kv[i] = ld128(rws, (h ? o1 : o0) + n * 16, QKV_OFF + ((i >= 3 ? 32 : 16) + pair) * (64 * 2560) + e * 1024);
      }
      const unsigned qso = QKV_OFF + (pair * 64 + 8 * QB + w) * 2560;
      q1 = __builtin_bit_cast(bf16x8, ld128(rws, n * 16 + h * 512, qso + e * 1536));
      q2u = ld64(rws, n * 16, qso + 1024 + 8 * e);
    }
    if constexpr (K > KVK) { constexpr int z = K - KVK - 1; ldB(z / 5, z % 5); }
    SB();
  });
  constexpr int NB0 = 79 - KVK;

  STAMP(6);
  REFRESH();
#pragma unroll
  for (int i = 0; i < 2; ++i) cs_write(cs2[i], EX2 + (2 * nlo + i) * 1024);
  if (w == 0 && lane == 0) *(LAS unsigned*)(size_t)CNT2 = 0u;
  wait_lgkm<0>();
  barrier();
#pragma unroll
  for (int i = 0; i < 2; ++i) acs[i] = lds_rd128(EX2 + (2 * nlo + i) * 1024 + l16);
  static_for<15 - NB0>([&](auto I) { constexpr int z = NB0 + I; ldB(z / 5, z % 5); });
  SB();
#pragma unroll
  for (int i = 0; i < 2; ++i) acc2[i] = mfma32(acs[i], negm, acc2[i]);
#pragma unroll
  for (int i = 0; i < 6; ++i) {
    const int c = (8 * i + w) - (i >= 3 ? 24 : 0);
    const int U0 = 2 * c, U1 = 2 * c + 1, b0 = (U0 * 43) >> 7, b1 = (U1 * 43) >> 7;
    const unsigned st = i >= 3 ? VS : KS;
    const unsigned o0 = b0 * st + (U0 - 3 * b0) * 512, o1 = b1 * st + (U1 - 3 * b1) * 512;
    *(LAS u32x4*)(size_t)((i >= 3 ? VO : KO) + (h ? o1 : o0) + n * 16) = kv[i];
  }
  if (w == 0 && lane < 48) {
    const int t = lane & 15, c = lane >> 4;
    const unsigned a = c == 0 ? KO + t * KS + 1536 : VO + t * VS + (c == 1 ? 1536 : 1792);
    *(LAS u32x2*)(size_t)a = c == 0 ? (u32x2){0x3f80u, 0u} : (u32x2){0x3f803f80u, 0x3f803f80u};
  }
  SB();
#pragma unroll
  for (int i = 0; i < 2; ++i) {
#pragma unroll
    for (int q = 0; q < 4; ++q) gu_pair(acc2[i], q);
    gu_store(3 * p + 1 + odd, 2 * nlo + i);
  }
  SB();
  wait_lgkm<0>();
  barrier();
  STAMP(7);
  REFRESH();

  const int jmin = QB == 0 ? 8 - w : 0;
  const unsigned ka1 = KO + w * KS + (e + h) * 512 + n * 16;
  const unsigned ka2 = KO + w * KS + (h ? 1536 : (e ? 8 : 1024) + n * 16);
  unsigned va0, va1;
  {
    const int g1 = (lane >> 4) & 1, q = (lane >> 2) & 3, pp = lane & 3;
    const int cc = 5 * e + (g1 ? 4 : pp);
    const int ua = cc >> 1, X = 4 * (ua & 3);
    const unsigned base = VO + w * VS + (ua - 2 * e) * 512 + (cc & 1) * 8;
    const bool ones = g1 && pp;
    va0 = ones ? VO + w * VS + 1536 : base + (((4 * h + q) ^ X) << 4);
    va1 = ones ? VO + w * VS + 1536 : base + (((8 + 4 * h + q) ^ X) << 4);
  }
  f32x16 o = (f32x16)(0.f);
  u32x4 pol2;
  {
    if (h) { q2u[0] = 0; q2u[1] = 0; }
    const bf16x8 k1d = lds_rd128(ka1 + 8 * KS);
    const s16x4 k2d = *(LAS s16x4*)(size_t)(ka2 + 8 * KS);
    const f32x16 a0 = mfma32(k1d, q1, (f32x16)(0.f));
    const f32x16 a1 = mfma8(k2d, __builtin_bit_cast(s16x4, q2u), a0);
    auto t = __builtin_amdgcn_permlane32_swap(__float_as_uint(a1[0]), __float_as_uint(a1[0]), false, false);
    if (h) q2u[0] = pk2(-__uint_as_float(t[0]), 0.f);
    const s16x4 q2 = __builtin_bit_cast(s16x4, q2u);
    auto pv = [&](const f32x16& s, int j, int mode) {
#pragma unroll
      for (int sh = 0; sh < 2; ++sh) {
        u32x4 pu;
#pragma unroll
        for (int r = 0; r < 4; ++r) {
          float p0 = __builtin_amdgcn_exp2f(s[8 * sh + 2 * r]), p1 = __builtin_amdgcn_exp2f(s[8 * sh + 2 * r + 1]);
          const int k0 = (2 * r & 3) + 8 * ((8 * sh + 2 * r) >> 2) + 4 * h;
          if (mode == 1) { p0 = k0 <= n ? p0 : 0.f; p1 = k0 + 1 <= n ? p1 : 0.f; }
          if (mode == 2) { p0 = k0 > n ? p0 : 0.f; p1 = k0 + 1 > n ? p1 : 0.f; }
          pu[r] = pk2(p0, p1);
        }
        const bf16x4 v0 = lds_tr(va0 + j * VS + sh * 256), v1 = lds_tr(va1 + j * VS + sh * 256);
        o = mfma32(__builtin_shufflevector(v0, v1, 0, 1, 2, 3, 4, 5, 6, 7), __builtin_bit_cast(bf16x8, pu), o);
      }
    };
    auto sc = [&](int j) {
      const bf16x8 k1 = lds_rd128(ka1 + j * KS);
      const s16x4 k2 = *(LAS s16x4*)(size_t)(ka2 + j * KS);
      return mfma8(k2, q2, mfma32(k1, q1, (f32x16)(0.f)));
    };
    f32x16 sa = mfma8(k2d, q2, a0), sb;
    static_for<9>([&](auto JJ) {
      constexpr int jj = JJ, j = 8 - jj;
      f32x16& cur = (jj & 1) ? sb : sa;
      f32x16& nxt = (jj & 1) ? sa : sb;
      if (j >= jmin) {
        if constexpr (j > 0) nxt = sc(j - 1);
        pv(cur, j, j == 8 ? 1 : (j == 0 ? 2 : 0));
      }
      SB();
      if constexpr (jj == 1) {
        wait_vm<0>();
        unsigned old = 0;
        if (lane == 0) old = __hip_atomic_fetch_add((LAS unsigned*)(size_t)CNT2, 1u, __ATOMIC_RELAXED, __HIP_MEMORY_SCOPE_WORKGROUP);
        if (__builtin_amdgcn_readfirstlane(old) == 7) st32(rws, lane == 0 ? 0u : OOB, F2_OFF + (mt * 16 + g) * 4, tag);
        static_for<5>([&](auto I) { ldB(3, I); });
      }
      if constexpr (jj == 4) pol2[0] = ld32(rws, (lane & 15) * 4, F2_OFF + (rg >> 1) * 64);
      if constexpr (jj == 6) {
        STAMP(8);
        if (!__all(pol2[0] == tag)) {
          for (unsigned sp = 0; sp < SPIN_CAP; ++sp) {
            __builtin_amdgcn_s_sleep(1);
            pol2[0] = ld32(rws, (lane & 15) * 4, F2_OFF + (rg >> 1) * 64);
            if (__all(pol2[0] == tag)) break;
          }
        }
        STAMP(9);
        SB();
        static_for<8>([&](auto I) { ldA(I / 4, I % 4); });
      }
      SB();
    });
    float amax = 0.f;
#pragma unroll
    for (int r = 0; r < 12; ++r) amax = __builtin_fmaxf(amax, __builtin_fabsf(o[r]));
    const bool okv = o[12] >= 9.0e-13f && o[12] <= 1.0e12f && amax <= 3.0e38f;
    if (!__all(okv)) {
      if (h) q2u[0] = 0;
      const s16x4 q2z = __builtin_bit_cast(s16x4, q2u);
      float mx = -__builtin_inff();
#pragma unroll 1
      for (int pass = 0; pass < 2; ++pass) {
        o = (f32x16)(0.f);
#pragma unroll 1
        for (int j = jmin; j < 9; ++j) {
          const bf16x8 k1 = lds_rd128(ka1 + j * KS);
          const s16x4 k2 = *(LAS s16x4*)(size_t)(ka2 + j * KS);
          f32x16 s = mfma8(k2, q2z, mfma32(k1, q1, (f32x16)(0.f)));
#pragma unroll
          for (int r = 0; r < 16; ++r) {
            const int k0 = (r & 3) + 8 * (r >> 2) + 4 * h;
            const bool dead = (j == 8 && k0 > n) || (j == 0 && k0 <= n);
            s[r] = dead ? -__builtin_inff() : s[r];
          }
          if (pass == 0) {
#pragma unroll
            for (int r = 0; r < 16; ++r) mx = __builtin_fmaxf(mx, s[r]);
          } else {
#pragma unroll
            for (int r = 0; r < 16; ++r) s[r] -= mx;
            pv(s, j, 0);
          }
        }
        if (pass == 0) {
          auto t2 = __builtin_amdgcn_permlane32_swap(__float_as_uint(mx), __float_as_uint(mx), false, false);
          mx = __builtin_fmaxf(__uint_as_float(t2[0]), __uint_as_float(t2[1]));
        }
      }
    }
  }
  {
    const float il = 1.f / o[12];
    const unsigned so = A2_OFF + (2 * (8 * QB + w) * 68 + 48) * 1024;
    const unsigned vb = (n >> 4) * (68 * 1024) + (n & 15) * 16;
    u32x4 val = {pk2(o[0] * il, o[1] * il), pk2(o[2] * il, o[3] * il), pk2(o[4] * il, o[5] * il), pk2(o[6] * il, o[7] * il)};
    st128(rws, vb + h * 256, so + (5 * pair + 2 * e) * 256, val);
    u32x2 v2 = {pk2(o[8] * il, o[9] * il), pk2(o[10] * il, o[11] * il)};
    st64(rws, h ? OOB : vb, so + (5 * pair + 4) * 256 + 8 * e, v2);
  }
  SB();
  STAMP(10);
  REFRESH();
  const int lg = lane; const unsigned lg16 = l16;

  f32x4 acc[4][5];
#pragma unroll
  for (int r = 0; r < 4; ++r)
#pragma unroll
    for (int c = 0; c < 5; ++c) acc[r][c] = (f32x4)(0.f);
  auto slot = [&](int s) {
#pragma unroll
    for (int r = 0; r < 4; ++r)
#pragma unroll
      for (int c = 0; c < 5; ++c)
        acc[r][c] = mfma16(__builtin_bit_cast(bf16x8, bq[s][c]), __builtin_bit_cast(bf16x8, aq[s][r]), acc[r][c]);
  };
  static_for<4>([&](auto I) { ldA(2, I); });
  SB(); slot(0); SB();
  static_for<4>([&](auto I) { ldA(3, I); });
  static_for<5>([&](auto I) { ldB(4, I); });
  SB(); slot(1); SB();
  wait_vm<13>();
  barrier();
  if (w == 0) st32(rws, fresh_lane() == 0 ? 0u : OOB, F3_OFF + (QB * 32 + head) * 4, tag);
  if (w >= 4) static_for<4>([&](auto I) { ldA(4, I); });
  static_for<5>([&](auto I) { ldB(5, I); });
  SB(); slot(2); SB();
  unsigned p3 = 0;
  if (w >= 4) static_for<4>([&](auto I) { ldA(5, I); });
  else p3 = ld32(rws, (lg & 31) * 4, F3_OFF + (rg >> 2) * 128);
  static_for<5>([&](auto I) { ldB(6, I); });
  SB(); slot(3); SB();
  STAMP(11);
  if (w >= 4) static_for<4>([&](auto I) { ldA(6, I); });
  else {
    if (!__all(p3 == tag)) {
      for (unsigned sp = 0; sp < SPIN_CAP; ++sp) {
        __builtin_amdgcn_s_sleep(1);
        p3 = ld32(rws, (lg & 31) * 4, F3_OFF + (rg >> 2) * 128);
        if (__all(p3 == tag)) break;
      }
    }
    SB();
    static_for<12>([&](auto I) { ldA(4 + I / 4, I % 4); });
  }
  STAMP(12);
  static_for<5>([&](auto I) { ldB(7, I); });
  SB(); slot(4); SB();
  static_for<4>([&](auto I) { ldA(7, I); });
  if (w < 4) static_for<5>([&](auto I) { ldB(8, I); });
  SB(); slot(5); SB();
  if (w < 4) static_for<4>([&](auto I) { ldA(8, I); });
  const int tok = lg & 15, g4 = lg >> 4, c0 = w < 4 ? 0 : 3;
  const unsigned oo = (64 * rg + 16 * (w & 3) + tok) * 1280 + ct * 160 + g4 * 8 + c0 * 32;
  u32x2 xres[3];
  xres[0] = *(const u32x2*)(x + oo); xres[1] = *(const u32x2*)(x + oo + 32);
  xres[2] = *(const u32x2*)(x + oo + (w < 4 ? 64 : 32));
  SB(); slot(6); SB();
  slot(7); SB();
  if (w < 4) slot(8);
  SB();
  STAMP(13);
#pragma unroll
  for (int r = 0; r < 4; ++r)
#pragma unroll
    for (int c = 0; c < 5; ++c) *(LAS f32x4*)(size_t)((w ^ 4) * 20480 + (r * 5 + c) * 1024 + lg16) = acc[r][c];
  wait_lgkm<0>();
  barrier();
  STAMP(14);
#pragma unroll
  for (int c = 0; c < 3; ++c) {
    if (c < 2 || w < 4) {
      const unsigned a = ((w & 3) * 5 + c0 + c) * 1024 + lg16;
      f32x4 t = ((lds_rdf4(a) + lds_rdf4(a + 20480)) + (lds_rdf4(a + 40960) + lds_rdf4(a + 61440))) +
                ((lds_rdf4(a + 81920) + lds_rdf4(a + 102400)) + (lds_rdf4(a + 122880) + lds_rdf4(a + 143360)));
      u32x2 val = {pk2(t[0] + lo_f(xres[c][0]), t[1] + hi_f(xres[c][0])), pk2(t[2] + lo_f(xres[c][1]), t[3] + hi_f(xres[c][1]))};
      *(u32x2*)(out + oo + c * 32) = val;
    }
  }
  if (b == 0 && w == 7) st32(rws, fresh_lane() == 0 ? 0u : OOB, GEN_OFF, tag);
  STAMP(15);
  STAMP_FLUSH(0);
}

extern "C" void kernel_launch(void* const* d_in, const int* in_sizes, int n_in, void* d_out, int out_size,
                              void* d_ws, size_t ws_size, hipStream_t stream) {
  (void)hipFuncSetAttribute((const void*)fused, hipFuncAttributeMaxDynamicSharedMemorySize, 163840);
  hipLaunchKernelGGL(fused, dim3(256), dim3(512), 163840, stream, (const char*)d_in[0], (const char*)d_in[1],
                     (const char*)d_in[2], (const char*)d_in[4], (const char*)d_in[5], (const char*)d_in[6],
                     (char*)d_ws, (char*)d_out);
}
```

```cpp
#include <hip/hip_runtime.h>
#include <hip/hip_bf16.h>
#include <stdint.h>

typedef __attribute__((ext_vector_type(2))) __bf16 bf16x2;
typedef __attribute__((ext_vector_type(4))) __bf16 bf16x4;
typedef __attribute__((ext_vector_type(8))) __bf16 bf16x8;
typedef __attribute__((ext_vector_type(4))) short s16x4;
typedef __attribute__((ext_vector_type(2))) float f32x2;
typedef __attribute__((ext_vector_type(4))) float f32x4;
typedef __attribute__((ext_vector_type(16))) float f32x16;
typedef __attribute__((ext_vector_type(2))) unsigned u32x2;
typedef __attribute__((ext_vector_type(4))) unsigned u32x4;
#define LAS __attribute__((address_space(3)))
#define DI __device__ __forceinline__

constexpr size_t QKV_OFF = 0;
constexpr size_t A2_OFF = 8u << 20;
constexpr size_t B2_OFF = 17u << 20;

#define STAMP_DECL
#define STAMP(S)
#define STAMP_FLUSH(K)

constexpr float LOG2E = 1.4426950408889634f;
constexpr float QSCALE = 0.22360679774997896f * LOG2E;

DI void dma16(unsigned voff, const void* sbase, unsigned lds) {
  asm volatile("s_mov_b32 m0, %2\n\ts_nop 0\n\tglobal_load_lds_dwordx4 %0, %1"
               :: "v"(voff), "s"(sbase), "s"(lds) : "memory");
}
template <int N> DI void wait_vm() { asm volatile("s_waitcnt vmcnt(%0)" :: "i"(N) : "memory"); }
template <int N, int M> DI void wait_vm_lgkm() { asm volatile("s_waitcnt vmcnt(%0) lgkmcnt(%1)" :: "i"(N), "i"(M) : "memory"); }
template <int M> DI void wait_lgkm() { asm volatile("s_waitcnt lgkmcnt(%0)" :: "i"(M) : "memory"); }
DI void barrier() { __builtin_amdgcn_s_barrier(); asm volatile("" ::: "memory"); }

DI bf16x8 lds_rd128(unsigned a) { return *(LAS bf16x8*)(size_t)a; }
DI f32x4 lds_rdf4(unsigned a) { return *(LAS f32x4*)(size_t)a; }
DI bf16x4 lds_tr(unsigned a) { return __builtin_amdgcn_ds_read_tr16_b64_v4bf16((LAS bf16x4*)(size_t)a); }
DI bf16x8 lds_trfrag(unsigned a, unsigned step) {
  bf16x4 lo = lds_tr(a), hi = lds_tr(a + step);
  return __builtin_shufflevector(lo, hi, 0, 1, 2, 3, 4, 5, 6, 7);
}
DI unsigned pk2(float a, float b) {
  f32x2 v = {a, b};
  return __builtin_bit_cast(unsigned, __builtin_convertvector(v, bf16x2));
}
DI float bfr(float a) { return (float)(__bf16)a; }
DI float lo_f(unsigned u) { return __uint_as_float(u << 16); }
DI float hi_f(unsigned u) { return __uint_as_float(u & 0xffff0000u); }
DI f32x16 mfma32(bf16x8 a, bf16x8 b, f32x16 c) { return __builtin_amdgcn_mfma_f32_32x32x16_bf16(a, b, c, 0, 0, 0); }

constexpr unsigned K1_WSLOT = 20480, K1_XOFF = 5 * K1_WSLOT, K1_XSLOT = 16384;
constexpr unsigned K1_EX = K1_XOFF + 3 * K1_XSLOT;
constexpr unsigned K1_LDS = 163840;

DI void acc_sum(float& s0, float& s1, bf16x8 f) {
  u32x4 u = __builtin_bit_cast(u32x4, f);
#pragma unroll
  for (int i = 0; i < 4; ++i) { s0 += lo_f(u[i]); s1 += hi_f(u[i]); }
}
DI void acc_stats(float& s, float& q, bf16x8 f) {
  u32x4 u = __builtin_bit_cast(u32x4, f);
#pragma unroll
  for (int i = 0; i < 4; ++i) {
    float a = lo_f(u[i]), b = hi_f(u[i]);
    s += a; q = __builtin_fmaf(a, a, q);
    s += b; q = __builtin_fmaf(b, b, q);
  }
}

template <int T>
DI void k1_body(const char* __restrict__ x, const char* __restrict__ wqkv, const char* __restrict__ wg,
                const char* __restrict__ wu, char* __restrict__ ws, int p, int odd, int mt, int w, int lane) {
  constexpr int NT = T == 0 ? 3 : 2;
  constexpr int NW = T == 0 ? 2 : 3;
  constexpr int G = NW + 1;
  STAMP_DECL; STAMP(0);
  const int wm = w & 1, nlo = (w >> 1) & 1, rot = wm ^ nlo, v = w & 3;
  const int h = lane >> 5, n = lane & 31;

  const char* wb[NW]; unsigned wv[NW]; unsigned wstep[NW];
  {
    const int r = lane >> 3, u = lane & 7;
#pragma unroll
    for (int j = 0; j < NW; ++j) {
      const int lc = T == 0 ? 3 + j : j;
      bool isq; int unit; int up;
      if (T == 0) { isq = false; unit = odd ? 3 * p + 2 : 3 * p + 1; up = j; }
      else if (odd) { isq = true; unit = 4 * p + 1 + j; if (unit > 29) unit = 29; up = 0; }
      else { isq = (j == 0); unit = (j == 0) ? 4 * p : 3 * p; up = (j == 2); }
      (void)lc;
      const unsigned stride = isq ? 3840u : 3072u;
      wb[j] = (isq ? wqkv : (up ? wu : wg)) + unit * 128 + (size_t)(8 * v) * stride;
      wstep[j] = 32 * stride;
      wv[j] = r * stride + ((u ^ (((r >> 1) & 1) << 2) ^ (up << 1)) << 4);
    }
  }
  const char* xb = x + (size_t)(128 * mt + 8 * w) * 1280;
  unsigned xv;
  { const int r = lane >> 3, u = lane & 7; xv = r * 1280 + ((u ^ (4 * (w & 1) + (r >> 1))) << 4); }
  const unsigned wdst = (T == 0 ? 3 * 4096 : 0) + v * 1024;
  const unsigned xdst = K1_XOFF + w * 1024;

  auto issueW = [&](unsigned slotoff) {
#pragma unroll
    for (int j = 0; j < NW; ++j) { dma16(wv[j], wb[j], slotoff + wdst + j * 4096); wb[j] += wstep[j]; }
  };
  auto issueX = [&](int stage, int part, unsigned slotoff) {
    dma16(xv, xb + part * (64 * 1280) + stage * 128, slotoff + xdst + part * 8192);
  };

  unsigned wa[NT];
  {
    const int cb = (lane >> 4) & 1, q = (lane >> 2) & 3, pp = lane & 3;
    const unsigned rowoff = (8 * h + q) * 128 + (pp & 1) * 8 + rot * 2048;
    const int sw = (q >> 1) << 2;
#pragma unroll
    for (int i = 0; i < NT; ++i) {
      bool isq; int lc, sel;
      if (T == 1) { isq = false; lc = 3; sel = 2 * nlo + i; }
      else if (odd) { isq = true; int j = 3 * nlo + i; lc = j >> 1; sel = j & 1; }
      else { isq = (i == 0); lc = (i == 0) ? 0 : 1; sel = (i == 0) ? nlo : 2 * nlo + i - 1; }
      unsigned a;
      if (isq) a = lc * 4096 + (((4 * sel + 2 * cb + (pp >> 1)) ^ sw) << 4);
      else a = (lc + cb) * 4096 + (((2 * sel + (pp >> 1)) ^ sw ^ (cb << 1)) << 4);
      wa[i] = a + rowoff;
    }
  }
  const int wd = rot ? -2048 : 2048;
  unsigned xa[2][2];
  {
    const int row = 64 * wm + n, sw = (row >> 1) & 7;
#pragma unroll
    for (int c = 0; c < 2; ++c)
#pragma unroll
      for (int j = 0; j < 2; ++j)
        xa[c][j] = K1_XOFF + row * 128 + (((4 * c + 2 * (j ^ rot) + h) ^ sw) << 4);
  }

  f32x16 acc[NT][2];
#pragma unroll
  for (int i = 0; i < NT; ++i) { acc[i][0] = (f32x16)(0.f); acc[i][1] = (f32x16)(0.f); }
  float cs0[NT], cs1[NT];
#pragma unroll
  for (int i = 0; i < NT; ++i) { cs0[i] = 0.f; cs1[i] = 0.f; }
  float rs[2] = {0.f, 0.f}, rq[2] = {0.f, 0.f};

  issueX(0, 0, 0);
  issueW(0 * K1_WSLOT); issueX(0, 1, 0);
  issueW(1 * K1_WSLOT); issueX(1, 0, K1_XSLOT);
  issueW(2 * K1_WSLOT); issueX(1, 1, K1_XSLOT);
  issueW(3 * K1_WSLOT); issueX(2, 0, 2 * K1_XSLOT);
  STAMP(1);
  wait_vm<3 * G>();
  barrier();
  STAMP(2);
  bf16x8 xf[2][2];
  bf16x8 wf0[NT];
#pragma unroll
  for (int j = 0; j < 2; ++j) { xf[j][0] = lds_rd128(xa[0][j]); xf[j][1] = lds_rd128(xa[0][j] + 4096); }
#pragma unroll
  for (int i = 0; i < NT; ++i) wf0[i] = lds_trfrag(wa[i], 512);

  unsigned wrd = 0;
  unsigned wwr = 4 * K1_WSLOT;
  unsigned xrd = 0;
  unsigned xwr = 2 * K1_XSLOT;

#pragma unroll 1
  for (int it = 0; it < 10; ++it) {
#pragma unroll
    for (int c = 0; c < 2; ++c) {
      if (c == 0) {
        if (it < 8) wait_vm_lgkm<2 * G, 2 * NT>();
        else if (it == 8) wait_vm_lgkm<2 * G - 1, 2 * NT>();
        else wait_vm_lgkm<0, 2 * NT>();
      } else {
        if (it < 8) wait_vm_lgkm<2 * G, 2 * NT>();
        else wait_vm_lgkm<NW, 2 * NT>();
      }
      barrier();
      const unsigned xnext = xrd == 2 * K1_XSLOT ? 0 : xrd + K1_XSLOT;
      if (it < 8) {
        issueW(wwr);
        if (c == 0) issueX(it + 2, 1, xwr);
        else if (it < 7) issueX(it + 3, 0, xrd);
      }
      wwr = wwr == 4 * K1_WSLOT ? 0 : wwr + K1_WSLOT;
      const unsigned wnext = wrd == 4 * K1_WSLOT ? 0 : wrd + K1_WSLOT;
      bf16x8 xn[2][2], wf1[NT], wn[NT];
#pragma unroll
      for (int i = 0; i < NT; ++i) wf1[i] = lds_trfrag(wa[i] + wrd + wd, 512);
      {
        const unsigned xo = c == 0 ? xrd : xnext;
#pragma unroll
        for (int j = 0; j < 2; ++j) {
          xn[j][0] = lds_rd128(xa[c ^ 1][j] + xo);
          xn[j][1] = lds_rd128(xa[c ^ 1][j] + xo + 4096);
        }
      }
#pragma unroll
      for (int i = 0; i < NT; ++i) {
        acc[i][0] = mfma32(wf0[i], xf[0][0], acc[i][0]);
        acc[i][1] = mfma32(wf0[i], xf[0][1], acc[i][1]);
        acc_sum(cs0[i], cs1[i], wf0[i]);
      }
      if (c == T) { acc_stats(rs[0], rq[0], xf[0][0]); acc_stats(rs[1], rq[1], xf[0][1]); }
      asm volatile("" ::: "memory");
#pragma unroll
      for (int i = 0; i < NT; ++i) wn[i] = lds_trfrag(wa[i] + wnext, 512);
#pragma unroll
      for (int i = 0; i < NT; ++i) {
        acc[i][0] = mfma32(wf1[i], xf[1][0], acc[i][0]);
        acc[i][1] = mfma32(wf1[i], xf[1][1], acc[i][1]);
      }
#pragma unroll
      for (int j = 0; j < 2; ++j) { xf[j][0] = xn[j][0]; xf[j][1] = xn[j][1]; }
#pragma unroll
      for (int i = 0; i < NT; ++i) wf0[i] = wn[i];
      wrd = wnext;
      if (c == 1) { xwr = xrd; xrd = xnext; }
    }
  }

  STAMP(3);
  constexpr unsigned CS = K1_EX, RS = K1_EX + 6144;
#pragma unroll
  for (int i = 0; i < NT; ++i)
    *(LAS float*)(size_t)(CS + ((((T * 2 + nlo) * 3 + i) * 4 + wm * 2 + h) * 32 + n) * 4) = cs0[i] + cs1[i];
#pragma unroll
  for (int rb = 0; rb < 2; ++rb) {
    auto a = __builtin_amdgcn_permlane32_swap(__float_as_uint(rs[rb]), __float_as_uint(rs[rb]), false, false);
    auto b = __builtin_amdgcn_permlane32_swap(__float_as_uint(rq[rb]), __float_as_uint(rq[rb]), false, false);
    f32x2 val = {__uint_as_float(a[0]) + __uint_as_float(a[1]), __uint_as_float(b[0]) + __uint_as_float(b[1])};
    if (h == 0) *(LAS f32x2*)(size_t)(RS + (((wm * 2 + rb) * 32 + n) * 4 + T * 2 + nlo) * 8) = val;
  }
  wait_lgkm<0>();
  barrier();
  STAMP(4);
  float mean[2], rstd[2];
#pragma unroll
  for (int rb = 0; rb < 2; ++rb) {
    f32x4 a = lds_rdf4(RS + ((wm * 2 + rb) * 32 + n) * 32), b = lds_rdf4(RS + ((wm * 2 + rb) * 32 + n) * 32 + 16);
    float s = (a[0] + a[2]) + (b[0] + b[2]), q = (a[1] + a[3]) + (b[1] + b[3]);
    float m = s * (1.f / 640.f);
    float var = __builtin_fmaxf(__builtin_fmaf(-m, m, q * (1.f / 640.f)), 0.f);
    float sd = bfr(__builtin_sqrtf(bfr(bfr(var) + 1e-5f)));
    mean[rb] = bfr(m);
    rstd[rb] = 1.f / sd;
  }

#pragma unroll
  for (int i = 0; i < NT; ++i) {
    float cs[16];
    {
      const unsigned cb = CS + ((T * 2 + nlo) * 3 + i) * 512 + h * 16;
#pragma unroll
      for (int a = 0; a < 4; ++a) {
        f32x4 t0 = lds_rdf4(cb + a * 32), t1 = lds_rdf4(cb + a * 32 + 128), t2 = lds_rdf4(cb + a * 32 + 256), t3 = lds_rdf4(cb + a * 32 + 384);
#pragma unroll
        for (int e = 0; e < 4; ++e) cs[4 * a + e] = (t0[e] + t1[e]) + (t2[e] + t3[e]);
      }
    }
    bool isq; int unit, sel;
    if (T == 1) { isq = false; unit = odd ? 3 * p + 2 : 3 * p + 1; sel = 2 * nlo + i; }
    else if (odd) { isq = true; int j = 3 * nlo + i; unit = 4 * p + 1 + (j >> 1); sel = j & 1; }
    else { isq = (i == 0); unit = (i == 0) ? 4 * p : 3 * p; sel = (i == 0) ? nlo : 2 * nlo + i - 1; }
    if (isq) {
      if (unit < 30) {
        const int cu0 = 8 * unit + 4 * sel;
        const float sc = cu0 < 80 ? QSCALE : 1.f;
#pragma unroll
        for (int rb = 0; rb < 2; ++rb) {
          const int tb = 4 * mt + 2 * wm + rb;
          const float r = rstd[rb] * sc, m = mean[rb];
          unsigned pkd[8];
#pragma unroll
          for (int e = 0; e < 8; ++e)
            pkd[e] = pk2(__builtin_fmaf(-m, cs[2 * e], acc[i][rb][2 * e]) * r,
                         __builtin_fmaf(-m, cs[2 * e + 1], acc[i][rb][2 * e + 1]) * r);
#pragma unroll
          for (int a = 0; a < 2; ++a) {
            auto s0 = __builtin_amdgcn_permlane32_swap(pkd[4 * a], pkd[4 * a + 2], false, false);
            auto s1 = __builtin_amdgcn_permlane32_swap(pkd[4 * a + 1], pkd[4 * a + 3], false, false);
            u32x4 val = {s0[0], s1[0], s0[1], s1[1]};
            const int cu = cu0 + 2 * a + h;
            const int P = (cu * 205) >> 10, u5 = cu - 5 * P;
            const int rr = P >= 32 ? (n ^ (4 * (u5 & 3))) : n;
            *(u32x4*)(ws + QKV_OFF + (size_t)((P * 64 + tb) * 2560 + u5 * 512 + rr * 16)) = val;
          }
        }
      }
    } else {
#pragma unroll
      for (int rb = 0; rb < 2; ++rb) {
        const int tb = 4 * mt + 2 * wm + rb;
        const float r = rstd[rb], m = mean[rb], rn = -LOG2E * r;
        float hv[8];
#pragma unroll
        for (int e = 0; e < 8; ++e) {
          float g = __builtin_fmaf(-m, cs[e], acc[i][rb][e]) * rn;
          float u = __builtin_fmaf(-m, cs[e + 8], acc[i][rb][e + 8]) * r;
          hv[e] = u * __builtin_amdgcn_rcpf(1.f + __builtin_amdgcn_exp2f(g));
        }
        u32x4 val = {pk2(hv[0], hv[1]), pk2(hv[2], hv[3]), pk2(hv[4], hv[5]), pk2(hv[6], hv[7])};
        const int rowblk = 2 * tb + (n >> 4), ks = 2 * unit + (sel >> 1), kg = 2 * (sel & 1) + h;
        *(u32x4*)(ws + A2_OFF + (size_t)(((rowblk * 68 + ks) * 64 + kg * 16 + (n & 15)) * 16)) = val;
      }
    }
  }
  STAMP(5);
  STAMP(6);
  STAMP_FLUSH(0);
}

__global__ __launch_bounds__(512, 2) void k1_gemm1(const char* __restrict__ x, const char* __restrict__ wqkv,
                                                   const char* __restrict__ wg, const char* __restrict__ wu,
                                                   char* __restrict__ ws) {
  const int b = blockIdx.x, xcd = b & 7, i = b >> 3;
  const int odd = i & 1, mt = i >> 1, p = xcd;
  const int w = __builtin_amdgcn_readfirstlane(threadIdx.x >> 6), lane = threadIdx.x & 63;
  if (w < 4) k1_body<0>(x, wqkv, wg, wu, ws, p, odd, mt, w, lane);
  else k1_body<1>(x, wqkv, wg, wu, ws, p, odd, mt, w, lane);
}

constexpr unsigned K2_V = 24576, K2_ONES = 49152, K2_LDS = 49152 + 64;

DI void retile_job(const char* __restrict__ wo, const char* __restrict__ wd, char* __restrict__ ws, int job, int lane) {
  const int ks = job / 10, G = job - 10 * ks;
  const int kg = lane >> 4, c4 = lane & 15;
  u32x2 r[8];
  if (ks < 48) {
    const char* src = wd + (size_t)(32 * ks + 16 * (kg >> 1) + 4 * (kg & 1)) * 1280 + G * 128 + c4 * 8;
#pragma unroll
    for (int j = 0; j < 8; ++j) r[j] = *(const u32x2*)(src + (8 * (j >> 2) + (j & 3)) * 1280);
  } else {
    const int u = 4 * (ks - 48) + kg, pr = (u * 205) >> 10, i5 = u - 5 * pr;
    const int row0 = i5 < 4 ? 40 * pr + 20 * (i5 >> 1) + 4 * (i5 & 1) : 40 * pr + 16;
    const int step = i5 < 4 ? 8 : 20;
    const char* src = wo + (size_t)row0 * 1280 + G * 128 + c4 * 8;
#pragma unroll
    for (int j = 0; j < 8; ++j) r[j] = *(const u32x2*)(src + (step * (j >> 2) + (j & 3)) * 1280);
  }
  char* dst = ws + B2_OFF + (size_t)((((4 * G + (c4 >> 2)) * 68 + ks) * 64 + kg * 16 + 4 * (c4 & 3)) * 16);
#pragma unroll
  for (int cc = 0; cc < 4; ++cc) {
    u32x4 o;
#pragma unroll
    for (int e = 0; e < 4; ++e) {
      unsigned a = r[2 * e][cc >> 1], b = r[2 * e + 1][cc >> 1];
      o[e] = (cc & 1) ? __builtin_amdgcn_perm(b, a, 0x07060302u) : __builtin_amdgcn_perm(b, a, 0x05040100u);
    }
    *(u32x4*)(dst + cc * 16) = o;
  }
}

__global__ __launch_bounds__(512, 2) void k2_attn(const char* __restrict__ wo, const char* __restrict__ wd,
                                                  char* __restrict__ ws) {
  const int b = blockIdx.x, xcd = b & 7, i = b >> 3;
  const int head = 4 * xcd + (i & 3), QB = i >> 2;
  const int w = __builtin_amdgcn_readfirstlane(threadIdx.x >> 6), lane = threadIdx.x & 63;
  const int pair = head >> 1, e = head & 1;
  const int h = lane >> 5, n = lane & 31;
  const char* qkv = ws + QKV_OFF;
  STAMP_DECL; STAMP(0);

#pragma unroll
  for (int j = 0; j < 6; ++j) {
    const int id = 8 * j + w;
    const int isv = id >= 24, c = isv ? id - 24 : id;
    const int o = c * 1024 + lane * 16, blk = o / 1536, within = o - blk * 1536;
    int tb = 8 * QB - 8 + blk; if (tb < 0) tb = 0;
    const unsigned voff = (unsigned)(((16 + 16 * isv + pair) * 64 + tb) * 2560 + e * 1024 + within);
    dma16(voff, qkv, id * 1024);
  }
  if (threadIdx.x < 4) *(LAS unsigned*)(size_t)(K2_ONES + threadIdx.x * 4) = 0x3f803f80u;
  const int qb = 8 * QB + w;
  const char* qsrc = qkv + (size_t)((pair * 64 + qb) * 2560 + n * 16);
  bf16x8 q1 = *(const bf16x8*)(qsrc + (e ? 3 + h : h) * 512);
  u32x2 q2u = *(const u32x2*)(qsrc + 1024 + 8 * e);
  if (h) { q2u[0] = 0; q2u[1] = 0; }
  const s16x4 q2 = __builtin_bit_cast(s16x4, q2u);

  {
    const int job = b * 8 + w;
    if (job < 680) retile_job(wo, wd, ws, job, lane);
  }

  STAMP(1);
  asm volatile("s_waitcnt vmcnt(0) lgkmcnt(0)" ::: "memory");
  barrier();
  STAMP(2);

  const unsigned ka1 = w * 1536 + (e + h) * 512 + n * 16;
  const unsigned ka2 = w * 1536 + (e ? 8 : 1024) + n * 16;
  const int jmin = QB == 0 ? 8 - w : 0;
  f32x16 s[9];
  const float NEG = -__builtin_inff();
#pragma unroll
  for (int j = 0; j < 9; ++j) {
    if (j >= jmin) {
      bf16x8 k1 = lds_rd128(ka1 + j * 1536);
      s16x4 k2 = *(LAS s16x4*)(size_t)(ka2 + j * 1536);
      f32x16 a = mfma32(k1, q1, (f32x16)(0.f));
      a = __builtin_amdgcn_mfma_f32_32x32x8bf16_1k(k2, q2, a, 0, 0, 0);
      if (j == 0) {
#pragma unroll
        for (int r = 0; r < 16; ++r) a[r] = ((r & 3) + 8 * (r >> 2) + 4 * h > n) ? a[r] : NEG;
      }
      if (j == 8) {
#pragma unroll
        for (int r = 0; r < 16; ++r) a[r] = ((r & 3) + 8 * (r >> 2) + 4 * h <= n) ? a[r] : NEG;
      }
      s[j] = a;
    } else {
      s[j] = (f32x16)(NEG);
    }
  }
  STAMP(3);
  float mx = s[8][0];
#pragma unroll
  for (int j = 0; j < 9; ++j)
#pragma unroll
    for (int r = 0; r < 16; ++r) mx = __builtin_fmaxf(mx, s[j][r]);
  {
    auto t = __builtin_amdgcn_permlane32_swap(__float_as_uint(mx), __float_as_uint(mx), false, false);
    mx = __builtin_fmaxf(__uint_as_float(t[0]), __uint_as_float(t[1]));
  }
  STAMP(4);
  unsigned va0, va1, vstepB, vstepS;
  {
    const int g1 = (lane >> 4) & 1, q = (lane >> 2) & 3, pp = lane & 3;
    const int cc = 5 * e + (g1 ? 4 : pp);
    const int ua = cc >> 1, X = 4 * (ua & 3);
    const unsigned base = K2_V + w * 1536 + (ua - 2 * e) * 512 + (cc & 1) * 8;
    const bool ones = g1 && pp;
    va0 = ones ? K2_ONES : base + (((4 * h + q) ^ X) << 4);
    va1 = ones ? K2_ONES : base + (((8 + 4 * h + q) ^ X) << 4);
    vstepB = ones ? 0 : 1536;
    vstepS = ones ? 0 : 256;
  }
  f32x16 o = (f32x16)(0.f);
#pragma unroll
  for (int j = 0; j < 9; ++j) {
    if (j >= jmin) {
#pragma unroll
      for (int sh = 0; sh < 2; ++sh) {
        u32x4 pu;
#pragma unroll
        for (int r = 0; r < 4; ++r)
          pu[r] = pk2(__builtin_amdgcn_exp2f(s[j][8 * sh + 2 * r] - mx), __builtin_amdgcn_exp2f(s[j][8 * sh + 2 * r + 1] - mx));
        bf16x4 v0 = lds_tr(va0 + j * vstepB + sh * vstepS), v1 = lds_tr(va1 + j * vstepB + sh * vstepS);
        bf16x8 vf = __builtin_shufflevector(v0, v1, 0, 1, 2, 3, 4, 5, 6, 7);
        o = mfma32(vf, __builtin_bit_cast(bf16x8, pu), o);
      }
    }
  }
  STAMP(5);
  const float il = 1.f / o[12];
  const int rowblk = 2 * qb + (n >> 4);
  char* a2 = ws + A2_OFF + (size_t)(rowblk * 68 * 1024 + (n & 15) * 16);
  {
    u32x4 val = {pk2(o[0] * il, o[1] * il), pk2(o[2] * il, o[3] * il), pk2(o[4] * il, o[5] * il), pk2(o[6] * il, o[7] * il)};
    const int u = 5 * pair + 2 * e + h;
    *(u32x4*)(a2 + (48 + (u >> 2)) * 1024 + (u & 3) * 256) = val;
    if (h == 0) {
      u32x2 v2 = {pk2(o[8] * il, o[9] * il), pk2(o[10] * il, o[11] * il)};
      const int uc = 5 * pair + 4;
      *(u32x2*)(a2 + (48 + (uc >> 2)) * 1024 + (uc & 3) * 256 + 8 * e) = v2;
    }
  }
  STAMP(6);
  STAMP(7);
  STAMP_FLUSH(1);
}

constexpr unsigned K3_LDS = 4 * 20480;

__global__ __launch_bounds__(256, 1) void k3_gemm2(const char* __restrict__ x, char* __restrict__ ws,
                                                   char* __restrict__ out) {
  const int b = blockIdx.x, xcd = b & 7, i = b >> 3;
  const int rg = 4 * xcd + (i >> 3), ct = i & 7;
  const int w = __builtin_amdgcn_readfirstlane(threadIdx.x >> 6), lane = threadIdx.x & 63;
  const char* ap = ws + A2_OFF + (size_t)((4 * rg * 68 + 17 * w) * 1024) + lane * 16;
  const char* bp = ws + B2_OFF + (size_t)((5 * ct * 68 + 17 * w) * 1024) + lane * 16;
  STAMP_DECL; STAMP(0);
  constexpr int PF = 4;
  bf16x8 af[PF][4], bf[PF][5];
  f32x4 acc[4][5];
#pragma unroll
  for (int r = 0; r < 4; ++r)
#pragma unroll
    for (int c = 0; c < 5; ++c) acc[r][c] = (f32x4)(0.f);
  auto load = [&](int ks, int slot) {
#pragma unroll
    for (int r = 0; r < 4; ++r) af[slot][r] = *(const bf16x8*)(ap + (r * 68 + ks) * 1024);
#pragma unroll
    for (int c = 0; c < 5; ++c) bf[slot][c] = *(const bf16x8*)(bp + (c * 68 + ks) * 1024);
  };
#pragma unroll
  for (int k = 0; k < PF - 1; ++k) load(k, k);
#pragma unroll
  for (int k = 0; k < 17; ++k) {
    if (k + PF - 1 < 17) load(k + PF - 1, (k + PF - 1) % PF);
    __builtin_amdgcn_sched_barrier(0);
#pragma unroll
    for (int r = 0; r < 4; ++r)
#pragma unroll
      for (int c = 0; c < 5; ++c)
        acc[r][c] = __builtin_amdgcn_mfma_f32_16x16x32_bf16(bf[k % PF][c], af[k % PF][r], acc[r][c], 0, 0, 0);
    __builtin_amdgcn_sched_barrier(0);
  }
  STAMP(1);
  const int tok = lane & 15, g = lane >> 4;
  const size_t ooff = (size_t)(64 * rg + 16 * w + tok) * 1280 + ct * 160 + g * 8;
  u32x2 xr[5];
#pragma unroll
  for (int c = 0; c < 5; ++c) xr[c] = *(const u32x2*)(x + ooff + c * 32);
#pragma unroll
  for (int r = 0; r < 4; ++r)
#pragma unroll
    for (int c = 0; c < 5; ++c) *(LAS f32x4*)(size_t)(w * 20480 + ((r * 5 + c) * 64 + lane) * 16) = acc[r][c];
  __syncthreads();
  STAMP(2);
#pragma unroll
  for (int c = 0; c < 5; ++c) {
    const unsigned a = ((w * 5 + c) * 64 + lane) * 16;
    f32x4 t = (lds_rdf4(a) + lds_rdf4(a + 20480)) + (lds_rdf4(a + 40960) + lds_rdf4(a + 61440));
    u32x2 val = {pk2(t[0] + lo_f(xr[c][0]), t[1] + hi_f(xr[c][0])), pk2(t[2] + lo_f(xr[c][1]), t[3] + hi_f(xr[c][1]))};
    *(u32x2*)(out + ooff + c * 32) = val;
  }
  STAMP(3);
  STAMP(4);
  STAMP_FLUSH(2);
}

extern "C" void kernel_launch(void* const* d_in, const int* in_sizes, int n_in, void* d_out, int out_size,
                              void* d_ws, size_t ws_size, hipStream_t stream) {
  const char* x = (const char*)d_in[0];
  const char* wqkv = (const char*)d_in[1];
  const char* wo = (const char*)d_in[2];
  const char* wg = (const char*)d_in[4];
  const char* wu = (const char*)d_in[5];
  const char* wd = (const char*)d_in[6];
  char* ws = (char*)d_ws;
  (void)hipFuncSetAttribute((const void*)k1_gemm1, hipFuncAttributeMaxDynamicSharedMemorySize, K1_LDS);
  (void)hipFuncSetAttribute((const void*)k3_gemm2, hipFuncAttributeMaxDynamicSharedMemorySize, K3_LDS);
  hipLaunchKernelGGL(k1_gemm1, dim3(256), dim3(512), K1_LDS, stream, x, wqkv, wg, wu, ws);
  hipLaunchKernelGGL(k2_attn, dim3(256), dim3(512), K2_LDS, stream, wo, wd, ws);
  hipLaunchKernelGGL(k3_gemm2, dim3(256), dim3(256), K3_LDS, stream, x, ws, (char*)d_out);
}
```

```cpp
#include <hip/hip_runtime.h>
#include <hip/hip_bf16.h>
#include <stdint.h>
#include <utility>
#define NOPF

typedef __attribute__((ext_vector_type(2))) __bf16 bf16x2;
typedef __attribute__((ext_vector_type(4))) __bf16 bf16x4;
typedef __attribute__((ext_vector_type(8))) __bf16 bf16x8;
typedef __attribute__((ext_vector_type(4))) short s16x4;
typedef __attribute__((ext_vector_type(2))) float f32x2;
typedef __attribute__((ext_vector_type(4))) float f32x4;
typedef __attribute__((ext_vector_type(16))) float f32x16;
typedef __attribute__((ext_vector_type(2))) unsigned u32x2;
typedef __attribute__((ext_vector_type(4))) unsigned u32x4;
#define LAS __attribute__((address_space(3)))
#define DI __device__ __forceinline__
#define SB() __builtin_amdgcn_sched_barrier(0)
#define IC(v) std::integral_constant<int, (v)>{}

constexpr unsigned QKV_OFF = 0;
constexpr unsigned A2_OFF = 7864320;
constexpr unsigned B2_OFF = 16777216;
constexpr unsigned CT_OFF = B2_OFF + 2785280;
constexpr unsigned F0_OFF = CT_OFF;
constexpr unsigned F1_OFF = CT_OFF + 1024;
constexpr unsigned F2_OFF = F1_OFF + 8192;
constexpr unsigned F3_OFF = F2_OFF + 2048;
constexpr unsigned GEN_OFF = F3_OFF + 4096;
constexpr unsigned GR_OFF = GEN_OFF + 1024;
constexpr unsigned WS_END = GR_OFF + 16384;
constexpr unsigned OOB = 0x80000000u;

#define STAMP_DECL
#define STAMP(S)
#define STAMP_FLUSH(K)

constexpr float LOG2E = 1.4426950408889634f;
constexpr float QSCALE = 0.22360679774997896f * LOG2E;

template <class F, int... I> DI void static_for_impl(F&& f, std::integer_sequence<int, I...>) { (f(std::integral_constant<int, I>{}), ...); }
template <int N, class F> DI void static_for(F&& f) { static_for_impl(f, std::make_integer_sequence<int, N>{}); }

DI void dma16(unsigned voff, const void* sbase, unsigned lds) {
  asm volatile("s_mov_b32 m0, %2\n\ts_nop 0\n\tglobal_load_lds_dwordx4 %0, %1"
               :: "v"(voff), "s"(sbase), "s"(lds) : "memory");
}
template <int N> DI void wait_vm() { asm volatile("s_waitcnt vmcnt(%0)" :: "i"(N) : "memory"); }
template <int N, int M> DI void wait_vm_lgkm() { asm volatile("s_waitcnt vmcnt(%0) lgkmcnt(%1)" :: "i"(N), "i"(M) : "memory"); }
template <int M> DI void wait_lgkm() { asm volatile("s_waitcnt lgkmcnt(%0)" :: "i"(M) : "memory"); }
DI void barrier() { __builtin_amdgcn_s_barrier(); asm volatile("" ::: "memory"); }

DI bf16x8 lds_rd128(unsigned a) { return *(LAS bf16x8*)(size_t)a; }
DI f32x4 lds_rdf4(unsigned a) { return *(LAS f32x4*)(size_t)a; }
DI bf16x4 lds_tr(unsigned a) { return __builtin_amdgcn_ds_read_tr16_b64_v4bf16((LAS bf16x4*)(size_t)a); }
DI bf16x8 lds_trfrag(unsigned a, unsigned step) {
  bf16x4 lo = lds_tr(a), hi = lds_tr(a + step);
  return __builtin_shufflevector(lo, hi, 0, 1, 2, 3, 4, 5, 6, 7);
}
DI unsigned pk2(float a, float b) {
  f32x2 v = {a, b};
  return __builtin_bit_cast(unsigned, __builtin_convertvector(v, bf16x2));
}
DI float bfr(float a) { return (float)(__bf16)a; }
DI float lo_f(unsigned u) { return __uint_as_float(u << 16); }
DI float hi_f(unsigned u) { return __uint_as_float(u & 0xffff0000u); }
DI f32x16 mfma32(bf16x8 a, bf16x8 b, f32x16 c) { return __builtin_amdgcn_mfma_f32_32x32x16_bf16(a, b, c, 0, 0, 0); }
DI f32x4 mfma16(bf16x8 a, bf16x8 b, f32x4 c) { return __builtin_amdgcn_mfma_f32_16x16x32_bf16(a, b, c, 0, 0, 0); }
DI f32x16 mfma8(s16x4 a, s16x4 b, f32x16 c) { return __builtin_amdgcn_mfma_f32_32x32x8bf16_1k(a, b, c, 0, 0, 0); }
typedef __amdgpu_buffer_rsrc_t rsrc_t;
DI rsrc_t mkrsrc(const void* p, unsigned n) { return __builtin_amdgcn_make_buffer_rsrc((void*)p, 0, n, 0x00020000); }
DI void st128(rsrc_t r, unsigned vo, unsigned so, u32x4 v) {
  __builtin_amdgcn_raw_buffer_store_b128(v, r, vo, so, 16);
  asm volatile("s_nop 1" :: "v"(v) : "memory");
}
DI void st64(rsrc_t r, unsigned vo, unsigned so, u32x2 v) { __builtin_amdgcn_raw_buffer_store_b64(v, r, vo, so, 16); }
DI void st32(rsrc_t r, unsigned vo, unsigned so, unsigned v) { __builtin_amdgcn_raw_buffer_store_b32(v, r, vo, so, 16); }
DI u32x4 ld128(rsrc_t r, unsigned vo, unsigned so) { return __builtin_amdgcn_raw_buffer_load_b128(r, vo, so, 16); }
DI u32x2 ld64(rsrc_t r, unsigned vo, unsigned so) { return __builtin_amdgcn_raw_buffer_load_b64(r, vo, so, 16); }
DI unsigned ld32(rsrc_t r, unsigned vo, unsigned so) { return __builtin_amdgcn_raw_buffer_load_b32(r, vo, so, 16); }
DI u32x4 mkdesc(const void* p, unsigned n) {
  const unsigned long long a = (unsigned long long)p;
  return (u32x4){(unsigned)a, (unsigned)(a >> 32) & 0xffffu, n, 0x00020000u};
}
template <int CTRL> DI float dpp_add(float v) {
  return v + __uint_as_float(__builtin_amdgcn_update_dpp(0, __float_as_uint(v), CTRL, 0xf, 0xf, true));
}
DI float wave_sum(float v) {
  v = dpp_add<0x111>(v); v = dpp_add<0x112>(v); v = dpp_add<0x114>(v); v = dpp_add<0x118>(v);
  const float a = __uint_as_float(__builtin_amdgcn_readlane(__float_as_uint(v), 15));
  const float b = __uint_as_float(__builtin_amdgcn_readlane(__float_as_uint(v), 31));
  const float c = __uint_as_float(__builtin_amdgcn_readlane(__float_as_uint(v), 47));
  const float d = __uint_as_float(__builtin_amdgcn_readlane(__float_as_uint(v), 63));
  return (a + b) + (c + d);
}
DI int fresh_lane() {
  int l; asm volatile("v_mbcnt_lo_u32_b32 %0, -1, 0\n\tv_mbcnt_hi_u32_b32 %0, -1, %0" : "=v"(l)); return l;
}
#define CSON true
DI float vmul(float a, float b) { float r; asm("v_mul_f32 %0, %1, %2" : "=v"(r) : "v"(a), "v"(b)); return r; }
DI float vmul_t(float a, float b) { float r; asm("s_nop 0\n\tv_mul_f32 %0, %1, %2" : "=v"(r) : "v"(a), "v"(b)); return r; }
DI unsigned vadd(unsigned a, unsigned c) { unsigned r; asm volatile("v_add_u32 %0, %2, %1" : "=v"(r) : "v"(a), "s"(c)); return r; }
#define ROTX 3
constexpr unsigned SPIN_CAP = 1u << 16;

constexpr unsigned XOFF = 98304, XSLOT = 16384, EXO = 16384, EX2 = 4096, RTO = 73728, RDY1 = 0;
constexpr unsigned xsl(int s) { return XOFF + (unsigned)(s & 3) * XSLOT; }
constexpr unsigned wsl1(int s) { return (unsigned)(s & 3) * 24576u; }
constexpr unsigned wsl2(int u) {
  return u == 0 ? 49152u : u == 1 ? 65536u : u == 2 ? 81920u : u == 3 ? 0u : u == 4 ? 16384u :
         u == 5 ? 32768u : u == 6 ? 49152u : u == 7 ? xsl(6) : u == 8 ? 65536u : xsl(7);
}
constexpr unsigned KS = 1552, VS = 1808, KO = 81920 + 16384, VO = KO + 16 * KS;
constexpr unsigned CNT2 = 80896, RDY2 = CNT2 + 4, RDY3 = CNT2 + 8, CNT3 = CNT2 + 12;
static_assert(VO + 16 * VS <= 163840, "lds");

#define GR_LD 34
#define FL1K 59
#define PL1K 64
#define KVK 71
#define FL2J 1
#define PL2J 5
constexpr int RT_RD = 1, RT_ST = 6, FL0K = 15, PL0K = 30, PFK = 38, E1K = 43;
struct Ops { short id[256]; short k[256]; short dma[256]; int n; };
constexpr int dma_at(int K, int slot) {
  const int G = (K - 3) >> 2, rel = (K - 3) & 3;
  if (K < 3) return -1;
  if (G <= 6) {
    const int s = G + 3;
    if (rel == 0) return slot == 0 ? 100 + 5 * s : 100 + 5 * s + 2;
    if (slot) return -1;
    return rel == 1 ? 100 + 5 * s + 1 : 100 + 5 * s + 1 + rel;
  }
  if (slot) return -1;
  if (K == 31 || K == 32) return 200 + (K - 31);
  if (K >= 35 && K <= 40) return 202 + (K - 35);
  if (K == 41 || K == 42) return 210 + (K - 41);
  if (K == 43 || K == 44) return 208 + (K - 43);
  if (K >= 47 && K <= 54) return 212 + (K - 47);
  return -1;
}
constexpr bool e1_store(bool odd, int K) {
  const int z = K - E1K;
  if (z < 0) return false;
  if (z < 4 || (odd && z < 12)) return z & 1;
  return !odd && (z == 4 + 7 || z == 4 + 15);
}
constexpr Ops make_ops(bool odd) {
  Ops o{}; int n = 0;
  auto add = [&](int id, int k, int d) { o.id[n] = (short)id; o.k[n] = (short)k; o.dma[n] = (short)d; ++n; };
  for (int i = 0; i < 5; ++i) add(100 + i, -3, 1);
  add(1, -3, 0); add(1, -3, 0); add(3, -3, 0); add(3, -3, 0); add(3, -3, 0);
  for (int i = 5; i < 15; ++i) add(100 + i, -3, 1);
  for (int K = 0; K < 80; ++K) {
    for (int s = 0; s < 2; ++s) if (dma_at(K, s) >= 0) add(dma_at(K, s), K, 1);
    if (K == RT_ST) for (int i = 0; i < 3; ++i) add(310, K, 0);
    if (K == PL0K) add(340, K, 0);
    if (K == GR_LD) add(320, K, 0);
    if (e1_store(odd, K)) add(330, K, 0);
  }
  o.n = n; return o;
}
constexpr Ops OPA = make_ops(true), OPB = make_ops(false);
constexpr bool stage_has(int id, int S) {
  return (id >= 100 && id < 200) ? (id - 100) / 5 <= S : (id >= 200 && id < 300) ? (id - 200) / 2 + 10 <= S : false;
}
constexpr int wait_stage(const Ops& o, int K, int S) {
  int issued = 0, last = -1;
  for (int i = 0; i < o.n; ++i) { if (o.k[i] < K) issued = i + 1; if (stage_has(o.id[i], S)) last = i; }
  const int w = issued - 1 - last; return w < 0 ? 0 : (w > 63 ? 63 : w);
}
constexpr int wait_mid(const Ops& o, int K, int id) {
  int issued = 0, last = -1;
  for (int i = 0; i < o.n; ++i) { if (o.k[i] < K || (o.k[i] == K && o.dma[i])) issued = i + 1; if (o.id[i] == id) last = i; }
  const int w = issued - 1 - last; return w < 0 ? 0 : (w > 63 ? 63 : w);
}

__global__ __launch_bounds__(512, 2) void fused(const char* __restrict__ x, const char* __restrict__ wqkv,
                                                const char* __restrict__ wo, const char* __restrict__ wg,
                                                const char* __restrict__ wu, const char* __restrict__ wd,
                                                char* __restrict__ ws, char* __restrict__ out) {
  const int b = blockIdx.x, xcd = b & 7, ii = b >> 3;
  const int mt = 8 * (xcd & 1) + (ii >> 2), g = 4 * (xcd >> 1) + (ii & 3), odd = g & 1, p = g >> 1;
  const int w = __builtin_amdgcn_readfirstlane(threadIdx.x >> 6);
  const int rgp = w & 3, nlo = w >> 2;
  int lane = threadIdx.x & 63, h = lane >> 5, n = lane & 31;
  unsigned l16 = lane * 16;
#define REFRESH() do { lane = fresh_lane(); h = lane >> 5; n = lane & 31; l16 = lane * 16; } while (0)
  const int r0 = ((mt & 7) + ((0x8530 >> (4 * (g & 3))) & 15) + xcd) % 10;
  const rsrc_t rws = mkrsrc(ws, WS_END);
  STAMP_DECL; STAMP(0);

  const char* wb[3]; unsigned wstep[3], wv[3];
  const char* gb; const char* ub; unsigned gv, uv;
  const char* xb = x + (size_t)(128 * mt + 16 * w) * 1280;
  unsigned xv0, xv1;
  { const int r = lane >> 3, u = lane & 7; xv0 = r * 1280 + ((u ^ (r >> 1)) << 4); xv1 = (r + 8) * 1280 + ((u ^ (4 + (r >> 1))) << 4); }
  {
    const int r = lane >> 3, u = lane & 7, sw = ((r >> 1) & 1) << 2;
    const unsigned vq = r * 3840 + ((u ^ sw) << 4);
    gv = r * 3072 + ((u ^ sw) << 4);
    uv = r * 3072 + ((u ^ sw ^ 2) << 4);
#pragma unroll
    for (int j = 0; j < 3; ++j) {
      const bool isq = odd || j == 0;
      int unit = odd ? 4 * p + 1 + j : (j == 0 ? 4 * p : 3 * p);
      if (isq && unit > 29) unit = 29;
      const unsigned stride = isq ? 3840u : 3072u;
      wb[j] = (isq ? wqkv : (j == 1 ? wg : wu)) + unit * 128 + (size_t)(8 * w) * stride;
      wstep[j] = 64 * stride;
      wv[j] = isq ? vq : (j == 1 ? gv : uv);
    }
    const int u2 = 3 * p + 1 + odd;
    gb = wg + u2 * 128 + (size_t)(8 * w) * 3072;
    ub = wu + u2 * 128 + (size_t)(8 * w) * 3072;
  }
  auto kst = [&](int pos) { int k = r0 + pos; return k >= 10 ? k - 10 : k; };
  auto issue = [&](auto IT) {
    constexpr int it = IT;
    if constexpr (it >= 200) {
      constexpr int u = (it - 200) >> 1, j = it & 1;
      dma16(j ? uv : gv, (j ? ub : gb) + (size_t)kst((u + 6) % 10) * (64 * 3072), wsl2(u) + j * 8192 + w * 1024);
    } else if constexpr (it >= 100) {
      constexpr int s = (it - 100) / 5, i = (it - 100) % 5;
      if constexpr (i < 2) dma16(i ? xv1 : xv0, xb + kst(s) * 128, xsl(s) + w * 2048 + i * 1024);
      else dma16(wv[i - 2], wb[i - 2] + (size_t)kst(s) * wstep[i - 2], wsl1(s) + (i - 2) * 8192 + w * 1024);
    }
  };
  static_for<5>([&](auto I) { issue(IC(100 + I)); });
  SB();
  STAMP(1);
  const int jok = b < 170, jb = jok ? b : b - 170;
  const int jG = (jb * 241) >> 12, jks = (jb - 17 * jG) * 4 + (w >> 1);
  {
    const int r = lane >> 3, u = lane & 7;
#pragma unroll
    for (int t = 0; t < 2; ++t) {
      const int kg = 2 * (w & 1) + t;
      const int uo = 4 * (jks - 48) + kg, pr = (uo * 205) >> 10, i5 = uo - 5 * pr;
      const int row0o = i5 < 4 ? 40 * pr + 20 * (i5 >> 1) + 4 * (i5 & 1) : 40 * pr + 16;
      const int row0 = jks < 48 ? 32 * jks + 16 * (kg >> 1) + 4 * (kg & 1) : row0o;
      const int step = (jks < 48 || i5 < 4) ? 8 : 20;
      dma16(((r & 3) + (r >> 2) * step) * 1280 + u * 16, (jks < 48 ? wd : wo) + row0 * 1280 + jG * 128, RTO + w * 2048 + t * 1024);
    }
  }
  u32x4 sx4; unsigned sx1, gen;
  const int srow = 128 * mt + 8 * g + w;
  {
    const char* rp = x + (size_t)srow * 1280;
    asm volatile("global_load_dwordx4 %0, %1, %2" : "=v"(sx4) : "v"(lane * 16), "s"(rp) : "memory");
    asm volatile("global_load_dword %0, %1, %2 offset:1024" : "=v"(sx1) : "v"(lane * 4), "s"(rp) : "memory");
    asm volatile("global_load_dword %0, %1, %2 sc1" : "=v"(gen) : "v"(0), "s"(ws + GEN_OFF) : "memory");
  }
  static_for<10>([&](auto I) { issue(IC(105 + I)); });

  unsigned wa1[3], wa2[2], xa[4];
  {
    const int cb = (lane >> 4) & 1, q = (lane >> 2) & 3, pp = lane & 3;
    const unsigned rowoff = (8 * h + q) * 128 + (pp & 1) * 8;
    const int sw = (q >> 1) << 2;
#pragma unroll
    for (int i = 0; i < 3; ++i) {
      const int j = 3 * nlo + i;
      const int piece = odd ? (j >> 1) : 0, sel = odd ? (j & 1) : nlo, t = 2 * nlo + i - 1;
      const unsigned aq = piece * 8192 + (((4 * sel + 2 * cb + (pp >> 1)) ^ sw) << 4);
      const unsigned ag = (1 + cb) * 8192 + (((2 * t + (pp >> 1)) ^ sw ^ (cb << 1)) << 4);
      wa1[i] = rowoff + ((odd || i == 0) ? aq : ag);
    }
#pragma unroll
    for (int i = 0; i < 2; ++i) wa2[i] = rowoff + cb * 8192 + (((2 * (2 * nlo + i) + (pp >> 1)) ^ sw ^ (cb << 1)) << 4);
    const int row = 32 * rgp + n, sx = (row >> 1) & 7;
#pragma unroll
    for (int j = 0; j < 4; ++j) xa[j] = XOFF + row * 128 + (((2 * j + h) ^ sx) << 4);
  }
  bf16x8 selv;
  {
    const int i = lane & 15, kg = lane >> 4;
    const unsigned o1 = ((i & 3) == 0 && ((i >> 2) & 1) == (kg & 1)) ? 0x3f803f80u : 0u;
    u32x4 t = {o1, o1, o1, o1};
    selv = __builtin_bit_cast(bf16x8, t);
  }
  SB();
  wait_vm<15>();
  barrier();
  STAMP(2);

  f32x16 acc1[3], acc2[2];
  f32x4 cs1[3], cs2[2];
#pragma unroll
  for (int i = 0; i < 3; ++i) { acc1[i] = (f32x16)(0.f); cs1[i] = (f32x4)(0.f); }
#pragma unroll
  for (int i = 0; i < 2; ++i) { acc2[i] = (f32x16)(0.f); cs2[i] = (f32x4)(0.f); }
  bf16x8 xr[56];
  bf16x8 wf[2][3];
  unsigned wcur[3];
  bf16x8 rt[2];
  u32x2 grn;
  unsigned tag, pf0, pf1;
  float smean;
  bf16x8 acs[3], negm;
  float rstd, sdv;
  float hv[8];
  unsigned pk[4];
  u32x4 kv[6], pol;
  unsigned pol1, rdy1;
  bf16x8 q1; u32x2 q2u;
  u32x4 bq[9][5], aq[9][4];

  const int head = 4 * xcd + (ii & 3), QB = ii >> 2, pair = head >> 1, e = head & 1;
  const int rg = 4 * xcd + (ii >> 3), ct = ii & 7;
  const int ks0 = w < 4 ? 4 * w : 8 * w - 16;
  const unsigned bso = B2_OFF + (unsigned)(5 * ct * 68) * 1024;
  const unsigned aso = A2_OFF + (unsigned)(4 * rg * 68) * 1024;
  auto ksof = [&](int s) { return (w < 4 && s >= 4) ? 44 + 5 * w + s : ks0 + s; };
  auto ldB = [&](int s, int c) { bq[s][c] = ld128(rws, l16, bso + (c * 68 + ksof(s)) * 1024); };
  auto ldA = [&](int s, int r) { aq[s][r] = ld128(rws, l16, aso + (r * 68 + ksof(s)) * 1024); };
  auto ldKV = [&](int i) {
    int t0 = 8 * QB - 8 + 2 * w; t0 = t0 < 0 ? 0 : t0;
    if (i < 6) {
      const unsigned so = QKV_OFF + (((i >= 3 ? 32 : 16) + pair) * 64 + t0) * 2560 + e * 1024;
      const int c = i % 3;
      kv[i] = c < 2 ? ld128(rws, l16, so + c * 2560) : ld128(rws, n * 16 + h * 2560, so + 1024);
    } else {
      const unsigned qso = QKV_OFF + (pair * 64 + 8 * QB + w) * 2560;
      if (i == 6) q1 = __builtin_bit_cast(bf16x8, ld128(rws, l16, qso + e * 1536));
      else q2u = ld64(rws, n * 16, qso + 1024 + 8 * e);
    }
  };
  auto pol1_ld = [&]() {
    const bool on = lane < 24 && (QB > 0 || lane >= 12);
    const unsigned v = ld32(rws, on ? lane * 4 : OOB, F1_OFF + (xcd * 8 + QB) * 128);
    return on ? v : tag;
  };

  auto rd = [&](auto KK) {
    constexpr int K = KK;
    if constexpr (K < 40) {
      constexpr int s = K >> 2, j = K & 3;
      xr[K] = lds_rd128(xa[j] + (xsl(s) - XOFF));
      if constexpr (j == 0 && (s & 3) == 3) {
#pragma unroll
        for (int i = 0; i < 3; ++i) wcur[i] = vadd(wa1[i], wsl1(s));
      }
#pragma unroll
      for (int i = 0; i < 3; ++i) wf[K & 1][i] = lds_trfrag(((s & 3) == 3 ? wcur[i] : wa1[i] + wsl1(s)) + j * 2048, 512);
    } else if constexpr (K < 80) {
      constexpr int u = (K - 40) >> 2, j = K & 3;
      if constexpr (u < 4) xr[K] = lds_rd128(xa[j] + (xsl(6 + u) - XOFF));
      constexpr bool far = wsl2(u) > 49152;
      if constexpr (j == 0 && far) {
#pragma unroll
        for (int i = 0; i < 2; ++i) wcur[i] = vadd(wa2[i], wsl2(u));
      }
#pragma unroll
      for (int i = 0; i < 2; ++i) wf[K & 1][i] = lds_trfrag((far ? wcur[i] : wa2[i] + wsl2(u)) + j * 2048, 512);
    }
  };
  auto cs_write = [&](f32x4 c, unsigned base) {
    const unsigned u0 = __float_as_uint(c[0]);
    const float r1 = c[0] - __uint_as_float(u0 & 0xffff0000u);
    const unsigned u1 = __float_as_uint(r1);
    const float r2 = r1 - __uint_as_float(u1 & 0xffff0000u);
    u32x2 v = {__builtin_amdgcn_perm(u1, u0, 0x07060302u), __float_as_uint(r2) >> 16};
    *(LAS u32x2*)(size_t)(base + ((rgp >> 1) * 32 + n) * 16 + (rgp & 1) * 8) = v;
  };
  auto qkv_a = [&](const f32x16& a16, int cu0, int a) {
    const float r = rstd * (cu0 < 80 ? QSCALE : 1.f);
#pragma unroll
    for (int i = 0; i < 4; ++i) pk[i] = pk2(vmul(a16[8 * a + 2 * i], r), vmul(a16[8 * a + 2 * i + 1], r));
  };
  auto qkv_b = [&](int cu0, int a, bool valid) {
    auto s0 = __builtin_amdgcn_permlane32_swap(pk[0], pk[2], false, false);
    auto s1 = __builtin_amdgcn_permlane32_swap(pk[1], pk[3], false, false);
    u32x4 val = {s0[0], s1[0], s0[1], s1[1]};
    const int c0 = cu0 + 2 * a, c1 = c0 + 1;
    const int P0 = (c0 * 205) >> 10, u0 = c0 - 5 * P0, P1 = (c1 * 205) >> 10, u1 = c1 - 5 * P1;
    const unsigned o0 = P0 * (64 * 2560) + u0 * 512, o1 = P1 * (64 * 2560) + u1 * 512;
    const unsigned x0 = c0 >= 160 ? (u0 & 3) * 64 : 0, x1 = c0 >= 160 ? (u1 & 3) * 64 : 0;
    const unsigned vo = __umul24(h, o1 - o0) + ((n * 16 ^ (valid ? x0 : OOB)) ^ __umul24(h, x0 ^ x1));
    st128(rws, vo, valid ? QKV_OFF + (4 * mt + rgp) * 2560 + o0 : 0u, val);
  };
  auto gu_one = [&](const f32x16& a16, int i) {
    const float ex = __builtin_amdgcn_exp2f(vmul(a16[i], -LOG2E * rstd));
    hv[i] = vmul_t(a16[i + 8], __builtin_amdgcn_rcpf(__builtin_fmaf(ex, sdv, sdv)));
  };
  auto gu_store = [&](int unit, int t) {
    u32x4 val = {pk2(hv[0], hv[1]), pk2(hv[2], hv[3]), pk2(hv[4], hv[5]), pk2(hv[6], hv[7])};
    const unsigned vo = (n >> 4) * (68 * 1024) + h * 256 + (n & 15) * 16;
    st128(rws, vo, A2_OFF + ((2 * (4 * mt + rgp) * 68 + 2 * unit + (t >> 1)) * 64 + 2 * (t & 1) * 16) * 16, val);
  };

  rd(IC(0));
  static_for<80>([&](auto KK) {
    constexpr int K = KK, S = K >> 2, j = K & 3;
    if constexpr (j == 3 && K <= FL1K) {
      if constexpr (K == FL1K) {
        asm volatile("s_waitcnt vmcnt(0) lgkmcnt(0)" : "+v"(pf0), "+v"(pf1) :: "memory");
      } else {
        constexpr int na = wait_stage(OPA, K, S + 1), nb = wait_stage(OPB, K, S + 1), nn = na < nb ? na : nb;
        wait_vm<nn>();
      }
      barrier();
      if constexpr (K == FL0K) { if (w == 0) st32(rws, fresh_lane() == 0 ? 0u : OOB, F0_OFF + b * 4, tag); }
      if constexpr (K == FL1K) {
        if (w == 0) {
          const int l = fresh_lane(), ui = l >> 2, hi = (l >> 1) & 1, qi = l & 1;
          const int u = odd ? 4 * p + 1 + ui : 4 * p;
          const int G = ((u * 4 * 205) >> 10) + hi;
          const int fu = (5 * G) >> 2;
          const int qb = (mt >> 1) + qi;
          const bool on = ui < (odd ? 3 : 1) && u < 30 && fu <= u && qb < 8;
          const unsigned a = ((G & 7) * 8 + qb) * 128 + ((mt - 2 * qb + 2) * 6 + (G >> 3) * 2 + (u - fu)) * 4;
          st32(rws, on ? a : OOB, F1_OFF, tag);
        }
      }
    }
    if constexpr (K == 41) { wait_lgkm<0>(); barrier(); }
    rd(IC(K + 1));
    if constexpr (dma_at(K, 0) >= 0) issue(IC(dma_at(K, 0)));
    if constexpr (dma_at(K, 1) >= 0) issue(IC(dma_at(K, 1)));
    if constexpr (K == 41) {
#pragma unroll
      for (int i = 0; i < 3; ++i) acs[i] = lds_rd128(EXO + (3 * nlo + i) * 1024 + l16);
    }
    if constexpr (K < 40) {
#pragma unroll
      for (int i = 0; i < 3; ++i) acc1[i] = mfma32(wf[K & 1][i], xr[K], acc1[i]);
      __builtin_amdgcn_sched_group_barrier(0x8, 1, 0); __builtin_amdgcn_sched_group_barrier(0x100, 3, 0);
      __builtin_amdgcn_sched_group_barrier(0x8, 1, 0); __builtin_amdgcn_sched_group_barrier(0x100, 2, 0);
      __builtin_amdgcn_sched_group_barrier(0x8, 1, 0); __builtin_amdgcn_sched_group_barrier(0x100, 2, 0);
      SB();
      if (CSON && rgp == j) {
#pragma unroll
        for (int i = 0; i < 3; ++i) cs1[i] = mfma16(selv, wf[K & 1][i], cs1[i]);
      }
    } else {
      constexpr int u = (K - 40) >> 2;
#pragma unroll
      for (int i = 0; i < 2; ++i) acc2[i] = mfma32(wf[K & 1][i], xr[u < 4 ? K : K - 56], acc2[i]);
      __builtin_amdgcn_sched_group_barrier(0x8, 1, 0); __builtin_amdgcn_sched_group_barrier(0x100, 3, 0);
      __builtin_amdgcn_sched_group_barrier(0x8, 1, 0); __builtin_amdgcn_sched_group_barrier(0x100, 5, 0);
      SB();
      if (CSON && rgp == j) {
#pragma unroll
        for (int i = 0; i < 2; ++i) cs2[i] = mfma16(selv, wf[K & 1][i], cs2[i]);
      }
    }
    SB();
    if constexpr (K == RT_RD) {
      asm volatile("s_waitcnt vmcnt(%3)" : "+v"(sx4), "+v"(sx1), "+v"(gen) : "i"(wait_mid(OPA, K, 3)) : "memory");
      const int q = (lane >> 2) & 3, pp = lane & 3;
      const unsigned a = RTO + w * 2048 + q * 128 + (lane >> 4) * 32 + pp * 8;
      rt[0] = lds_trfrag(a, 512); rt[1] = lds_trfrag(a + 1024, 512);
      tag = __builtin_amdgcn_readfirstlane(gen) * 0x9E3779B1u + 0x7F4A7C15u;
      float s = lo_f(sx1) + hi_f(sx1);
#pragma unroll
      for (int i = 0; i < 4; ++i) s += lo_f(sx4[i]) + hi_f(sx4[i]);
      smean = wave_sum(s) * (1.f / 640.f);
    }
    if constexpr (K == RT_RD + 1) asm volatile("s_waitcnt lgkmcnt(0)" : "+v"(rt[0]), "+v"(rt[1]) :: "memory");
    if constexpr (K == RT_RD + 3) {
      float q = 0.f;
#pragma unroll
      for (int i = 0; i < 5; ++i) {
        const unsigned u = i < 4 ? sx4[i] : sx1;
        const float d0 = lo_f(u) - smean, d1 = hi_f(u) - smean;
        q = __builtin_fmaf(d0, d0, q); q = __builtin_fmaf(d1, d1, q);
      }
      const float var = wave_sum(q) * (1.f / 640.f);
      grn[0] = pk2(smean, __builtin_amdgcn_sqrtf(bfr(bfr(var) + 1e-5f)));
      grn[1] = tag;
    }
    if constexpr (K == RT_ST) {
      REFRESH();
      st64(rws, lane == 0 ? 0u : OOB, GR_OFF + srow * 8, grn);
      const unsigned vo = jok ? (lane >> 4) * (68 * 1024) + (lane & 15) * 16 : OOB;
#pragma unroll
      for (int t = 0; t < 2; ++t)
        st128(rws, vo, B2_OFF + (4 * jG * 68 + jks) * 1024 + (2 * (w & 1) + t) * 256, __builtin_bit_cast(u32x4, rt[t]));
    }
    if constexpr (K == PL0K) {
      REFRESH();
      asm volatile("buffer_load_dwordx4 %0, %1, %2, %3 offen sc1" : "=v"(pol) : "v"(w == 7 ? l16 : OOB), "s"(mkdesc(ws, WS_END)), "s"(F0_OFF) : "memory");
    }
    if constexpr (K == GR_LD) {
      REFRESH();
      static_assert(wait_mid(OPA, K, 340) == wait_mid(OPB, K, 340), "");
      asm volatile("s_waitcnt vmcnt(%1)" : "+v"(pol) : "i"(wait_mid(OPA, K, 340)) : "memory");
      if (w == 7) {
        auto ok = [&]() { return pol[0] == tag && pol[1] == tag && pol[2] == tag && pol[3] == tag; };
        if (!__all(ok())) {
#pragma unroll 1
          for (unsigned sp = 0; sp < SPIN_CAP; ++sp) {
            __builtin_amdgcn_s_sleep(4);
            pol = ld128(rws, l16, F0_OFF);
            if (__all(ok())) break;
          }
        }
      }
      asm volatile("buffer_load_dwordx2 %0, %1, %2, %3 offen sc1" : "=v"(grn) : "v"(n * 8), "s"(mkdesc(ws, WS_END)), "s"(GR_OFF + (128 * mt + 32 * rgp) * 8) : "memory");
    }
    if constexpr (K == PFK) { pf0 = 0; pf1 = 0; }
    if constexpr (K == 40) {
      STAMP(3);
      REFRESH();
#pragma unroll
      for (int i = 0; i < 3; ++i) cs_write(cs1[i], EXO + (3 * nlo + i) * 1024);
      static_assert(wait_mid(OPA, K, 320) == wait_mid(OPB, K, 320), "");
      asm volatile("s_waitcnt vmcnt(%1)" : "+v"(grn) : "i"(wait_mid(OPA, K, 320)) : "memory");
      if (!__all(grn[1] == tag)) {
#pragma unroll 1
        for (unsigned sp = 0; sp < SPIN_CAP; ++sp) {
          __builtin_amdgcn_s_sleep(4);
          grn = ld64(rws, n * 8, GR_OFF + (128 * mt + 32 * rgp) * 8);
          if (__all(grn[1] == tag)) break;
        }
      }
      const unsigned nm = ((grn[0] & 0xffffu) ^ 0x8000u) * 0x10001u;
      u32x4 t = {nm, nm, nm, nm};
      negm = __builtin_bit_cast(bf16x8, t);
      sdv = hi_f(grn[0]);
      rstd = __builtin_amdgcn_rcpf(sdv);
    }
    if constexpr (K == 42) {
#pragma unroll
      for (int i = 0; i < 3; ++i) acc1[i] = mfma32(acs[i], negm, acc1[i]);
    }
    if constexpr (K >= E1K && K < E1K + 4) {
      constexpr int z = K - E1K;
      const int j0 = 3 * nlo;
      const int unit = odd ? 4 * p + 1 + (j0 >> 1) : 4 * p, sel = odd ? (j0 & 1) : nlo;
      if constexpr (z & 1) qkv_b(8 * unit + 4 * sel, z >> 1, unit < 30);
      else qkv_a(acc1[0], 8 * unit + 4 * sel, z >> 1);
    }
    if constexpr (K >= E1K + 4 && K < E1K + 20) {
      constexpr int z = K - E1K - 4;
      if (odd) {
        if constexpr (z < 8) {
          const int jj = 3 * nlo + 1 + (z >> 2), unit = 4 * p + 1 + (jj >> 1);
          if constexpr (z & 1) qkv_b(8 * unit + 4 * (jj & 1), (z >> 1) & 1, unit < 30);
          else qkv_a(acc1[1 + (z >> 2)], 8 * unit + 4 * (jj & 1), (z >> 1) & 1);
        }
      } else {
        gu_one(acc1[1 + (z >> 3)], z & 7);
        if constexpr ((z & 7) == 7) gu_store(3 * p, 2 * nlo + (z >> 3));
      }
    }
    if constexpr (K == 56) { if (w == 7) *(LAS unsigned*)(size_t)RDY1 = 0u; }
    if constexpr (K == PL1K) { REFRESH(); if (w == 7) pol1 = pol1_ld(); }
    if constexpr (K == KVK - 3) { if (w == 7 && __all(pol1 == tag)) *(LAS unsigned*)(size_t)RDY1 = 1u; }
    if constexpr (K == KVK - 2) rdy1 = *(volatile LAS unsigned*)(size_t)RDY1;
    if constexpr (K == KVK - 1) { STAMP(4); rdy1 = __builtin_amdgcn_readfirstlane(rdy1); }
    if constexpr (K >= KVK && K < KVK + 8) { if (rdy1) ldKV(K - KVK); }
    if constexpr (K >= FL1K && K < FL1K + 20) { constexpr int z = K - FL1K; ldB(z / 5, z % 5); }
    SB();
  });

  STAMP(5);
  REFRESH();
#pragma unroll
  for (int i = 0; i < 2; ++i) cs_write(cs2[i], EX2 + (2 * nlo + i) * 1024);
  if (w == 7 && !rdy1) {
#pragma unroll 1
    for (unsigned sp = 0; sp < SPIN_CAP; ++sp) {
      pol1 = pol1_ld();
      if (__all(pol1 == tag)) break;
      __builtin_amdgcn_s_sleep(4);
    }
  }
  wait_lgkm<0>();
  barrier();
  STAMP(6);
  if (!rdy1) static_for<8>([&](auto I) { ldKV(I); });
#pragma unroll
  for (int i = 0; i < 2; ++i) acs[i] = lds_rd128(EX2 + (2 * nlo + i) * 1024 + l16);
  SB();
#pragma unroll
  for (int i = 0; i < 2; ++i) acc2[i] = mfma32(acs[i], negm, acc2[i]);
  if (w == 0 && lane < 50) {
    const int t = lane & 15, c = lane >> 4;
    const unsigned a = c == 0 ? KO + t * KS + 1536 : c == 3 ? CNT2 + t * 8 : VO + t * VS + (c == 1 ? 1536 : 1792);
    *(LAS u32x2*)(size_t)a = c == 0 ? (u32x2){0x3f80u, 0u} : c == 3 ? (u32x2){0u, 0u} : (u32x2){0x3f803f80u, 0x3f803f80u};
  }
  SB();
#pragma unroll
  for (int i = 0; i < 2; ++i) {
#pragma unroll
    for (int q = 0; q < 8; ++q) gu_one(acc2[i], q);
    gu_store(3 * p + 1 + odd, 2 * nlo + i);
  }
  SB();
#pragma unroll
  for (int i = 0; i < 6; ++i) {
    const unsigned st = i >= 3 ? VS : KS, c = i % 3;
    const unsigned base = (i >= 3 ? VO : KO) + 2 * w * st;
    *(LAS u32x4*)(size_t)(c < 2 ? base + c * st + l16 : base + 1024 + h * st + n * 16) = kv[i];
  }
  wait_lgkm<0>();
  barrier();
  STAMP(7);
  REFRESH();

  const int jmin = QB == 0 ? 8 - w : 0;
  const unsigned ka1 = KO + w * KS + (e + h) * 512 + n * 16;
  const unsigned ka2 = KO + w * KS + (h ? 1536 : (e ? 8 : 1024) + n * 16);
  unsigned va0, va1;
  {
    const int g1 = (lane >> 4) & 1, q = (lane >> 2) & 3, pp = lane & 3;
    const int cc = 5 * e + (g1 ? 4 : pp);
    const int ua = cc >> 1, X = 4 * (ua & 3);
    const unsigned base = VO + w * VS + (ua - 2 * e) * 512 + (cc & 1) * 8;
    const bool ones = g1 && pp;
    va0 = ones ? VO + w * VS + 1536 : base + (((4 * h + q) ^ X) << 4);
    va1 = ones ? VO + w * VS + 1536 : base + (((8 + 4 * h + q) ^ X) << 4);
  }
  f32x16 o = (f32x16)(0.f);
  unsigned pol2 = 0, rdy2 = 0;
  {
    if (h) { q2u[0] = 0; q2u[1] = 0; }
    const bf16x8 k1d = lds_rd128(ka1 + 8 * KS);
    const s16x4 k2d = *(LAS s16x4*)(size_t)(ka2 + 8 * KS);
    const f32x16 a0 = mfma32(k1d, q1, (f32x16)(0.f));
    const f32x16 a1 = mfma8(k2d, __builtin_bit_cast(s16x4, q2u), a0);
    auto t = __builtin_amdgcn_permlane32_swap(__float_as_uint(a1[0]), __float_as_uint(a1[0]), false, false);
    if (h) q2u[0] = pk2(-__uint_as_float(t[0]), 0.f);
    const s16x4 q2 = __builtin_bit_cast(s16x4, q2u);
    auto pv = [&](const f32x16& s, int j, int mode) {
#pragma unroll
      for (int sh = 0; sh < 2; ++sh) {
        u32x4 pu;
#pragma unroll
        for (int r = 0; r < 4; ++r) {
          float p0 = __builtin_amdgcn_exp2f(s[8 * sh + 2 * r]), p1 = __builtin_amdgcn_exp2f(s[8 * sh + 2 * r + 1]);
          const int k0 = (2 * r & 3) + 8 * ((8 * sh + 2 * r) >> 2) + 4 * h;
          if (mode == 1) { p0 = k0 <= n ? p0 : 0.f; p1 = k0 + 1 <= n ? p1 : 0.f; }
          if (mode == 2) { p0 = k0 > n ? p0 : 0.f; p1 = k0 + 1 > n ? p1 : 0.f; }
          pu[r] = pk2(p0, p1);
        }
        const bf16x4 v0 = lds_tr(va0 + j * VS + sh * 256), v1 = lds_tr(va1 + j * VS + sh * 256);
        o = mfma32(__builtin_shufflevector(v0, v1, 0, 1, 2, 3, 4, 5, 6, 7), __builtin_bit_cast(bf16x8, pu), o);
      }
    };
    auto sc = [&](int j) {
      const bf16x8 k1 = lds_rd128(ka1 + j * KS);
      const s16x4 k2 = *(LAS s16x4*)(size_t)(ka2 + j * KS);
      return mfma8(k2, q2, mfma32(k1, q1, (f32x16)(0.f)));
    };
    f32x16 sa = mfma8(k2d, q2, a0), sb;
    static_for<9>([&](auto JJ) {
      constexpr int jj = JJ, j = 8 - jj;
      f32x16& cur = (jj & 1) ? sb : sa;
      f32x16& nxt = (jj & 1) ? sa : sb;
      if (j >= jmin) {
        if constexpr (j > 0) nxt = sc(j - 1);
        pv(cur, j, j == 8 ? 1 : (j == 0 ? 2 : 0));
      }
      SB();
      if constexpr (jj == FL2J) {
        wait_vm<0>();
        unsigned old = 0;
        if (lane == 0) old = __hip_atomic_fetch_add((LAS unsigned*)(size_t)CNT2, 1u, __ATOMIC_RELAXED, __HIP_MEMORY_SCOPE_WORKGROUP);
        if (__builtin_amdgcn_readfirstlane(old) == 7) st32(rws, lane == 0 ? 0u : OOB, F2_OFF + mt * 128 + g * 4, tag);
      }
      if constexpr (jj == PL2J) { if (w == 7) pol2 = ld32(rws, (lane & 15) * 4, F2_OFF + (rg >> 1) * 128); }
      if constexpr (jj == PL2J + 1) { if (w == 7 && __all(pol2 == tag)) *(LAS unsigned*)(size_t)RDY2 = 1u; }
      if constexpr (jj == PL2J + 2) {
        rdy2 = __builtin_amdgcn_readfirstlane(*(volatile LAS unsigned*)(size_t)RDY2);
        STAMP(8);
        if (rdy2) static_for<8>([&](auto I) { ldA(I / 4, I % 4); });
      }
      SB();
    });
    float amax = 0.f;
#pragma unroll
    for (int r = 0; r < 12; ++r) amax = __builtin_fmaxf(amax, __builtin_fabsf(o[r]));
    const bool okv = o[12] >= 9.0e-13f && o[12] <= 1.0e12f && amax <= 3.0e38f;
    if (!__all(okv)) {
      if (h) q2u[0] = 0;
      const s16x4 q2z = __builtin_bit_cast(s16x4, q2u);
      float mx = -__builtin_inff();
#pragma unroll 1
      for (int pass = 0; pass < 2; ++pass) {
        o = (f32x16)(0.f);
#pragma unroll 1
        for (int j = jmin; j < 9; ++j) {
          const bf16x8 k1 = lds_rd128(ka1 + j * KS);
          const s16x4 k2 = *(LAS s16x4*)(size_t)(ka2 + j * KS);
          f32x16 s = mfma8(k2, q2z, mfma32(k1, q1, (f32x16)(0.f)));
#pragma unroll
          for (int r = 0; r < 16; ++r) {
            const int k0 = (r & 3) + 8 * (r >> 2) + 4 * h;
            const bool dead = (j == 8 && k0 > n) || (j == 0 && k0 <= n);
            s[r] = dead ? -__builtin_inff() : s[r];
          }
          if (pass == 0) {
#pragma unroll
            for (int r = 0; r < 16; ++r) mx = __builtin_fmaxf(mx, s[r]);
          } else {
#pragma unroll
            for (int r = 0; r < 16; ++r) s[r] -= mx;
            pv(s, j, 0);
          }
        }
        if (pass == 0) {
          auto t2 = __builtin_amdgcn_permlane32_swap(__float_as_uint(mx), __float_as_uint(mx), false, false);
          mx = __builtin_fmaxf(__uint_as_float(t2[0]), __uint_as_float(t2[1]));
        }
      }
    }
  }
  {
    const float il = 1.f / o[12];
    const unsigned so = A2_OFF + (2 * (8 * QB + w) * 68 + 48) * 1024;
    const unsigned vb = (n >> 4) * (68 * 1024) + (n & 15) * 16;
    u32x4 val = {pk2(o[0] * il, o[1] * il), pk2(o[2] * il, o[3] * il), pk2(o[4] * il, o[5] * il), pk2(o[6] * il, o[7] * il)};
    st128(rws, vb + h * 256, so + (5 * pair + 2 * e) * 256, val);
    u32x2 v2 = {pk2(o[8] * il, o[9] * il), pk2(o[10] * il, o[11] * il)};
    st64(rws, h ? OOB : vb, so + (5 * pair + 4) * 256 + 8 * e, v2);
  }
  SB();
  STAMP(10);
  REFRESH();
  const int lg = lane; const unsigned lg16 = l16;

  f32x4 acc[4][5];
#pragma unroll
  for (int r = 0; r < 4; ++r)
#pragma unroll
    for (int c = 0; c < 5; ++c) acc[r][c] = (f32x4)(0.f);
  auto slot = [&](int s) {
#pragma unroll
    for (int r = 0; r < 4; ++r)
#pragma unroll
      for (int c = 0; c < 5; ++c)
        acc[r][c] = mfma16(__builtin_bit_cast(bf16x8, bq[s][c]), __builtin_bit_cast(bf16x8, aq[s][r]), acc[r][c]);
  };
  auto lds_wait = [&](unsigned a, unsigned v) {
#pragma unroll 1
    for (unsigned sp = 0; sp < 64 * SPIN_CAP; ++sp) {
      if (__builtin_amdgcn_readfirstlane(*(volatile LAS unsigned*)(size_t)a) >= v) break;
      __builtin_amdgcn_s_sleep(1);
    }
  };
  if (rdy2) slot(0);
  SB();
  {
    wait_vm<0>();
    unsigned old = 0;
    if (lg == 0) old = __hip_atomic_fetch_add((LAS unsigned*)(size_t)CNT3, 1u, __ATOMIC_RELAXED, __HIP_MEMORY_SCOPE_WORKGROUP);
    if (__builtin_amdgcn_readfirstlane(old) == 7) st32(rws, lg < 4 ? lg * 128 : OOB, F3_OFF + QB * 512 + head * 4, tag);
  }
  if (!rdy2) {
    if (w == 7) {
#pragma unroll 1
      for (unsigned sp = 0; sp < SPIN_CAP; ++sp) {
        pol2 = ld32(rws, (lg & 15) * 4, F2_OFF + (rg >> 1) * 128);
        if (__all(pol2 == tag)) break;
        __builtin_amdgcn_s_sleep(4);
      }
      *(LAS unsigned*)(size_t)RDY2 = 1u;
    } else lds_wait(RDY2, 1);
    SB();
    static_for<8>([&](auto I) { ldA(I / 4, I % 4); });
  }
  STAMP(9);
  static_for<8>([&](auto I) { ldA(2 + I / 4, I % 4); });
  SB();
  if (!rdy2) slot(0);
  SB();
  static_for<5>([&](auto I) { ldB(4, I); });
  SB(); slot(1); SB();
  if (w >= 4) static_for<4>([&](auto I) { ldA(4, I); });
  static_for<5>([&](auto I) { ldB(5, I); });
  SB(); slot(2); SB();
  unsigned p3 = 0;
  if (w >= 4) static_for<4>([&](auto I) { ldA(5, I); });
  else if (w == 0) p3 = ld32(rws, (lg & 31) * 4, F3_OFF + rg * 128);
  static_for<5>([&](auto I) { ldB(6, I); });
  SB(); slot(3); SB();
  STAMP(11);
  if (w >= 4) static_for<4>([&](auto I) { ldA(6, I); });
  else {
    if (w == 0) {
      if (!__all(p3 == tag)) {
#pragma unroll 1
        for (unsigned sp = 0; sp < SPIN_CAP; ++sp) {
          p3 = ld32(rws, (lg & 31) * 4, F3_OFF + rg * 128);
          if (__all(p3 == tag)) break;
          __builtin_amdgcn_s_sleep(4);
        }
      }
      *(LAS unsigned*)(size_t)RDY3 = 1u;
    } else lds_wait(RDY3, 1);
    SB();
    static_for<12>([&](auto I) { ldA(4 + I / 4, I % 4); });
  }
  STAMP(12);
  static_for<5>([&](auto I) { ldB(7, I); });
  SB(); slot(4); SB();
  static_for<4>([&](auto I) { ldA(7, I); });
  if (w < 4) static_for<5>([&](auto I) { ldB(8, I); });
  SB(); slot(5); SB();
  if (w < 4) static_for<4>([&](auto I) { ldA(8, I); });
  const int tok = lg & 15, g4 = lg >> 4, c0 = w < 4 ? 0 : 3;
  const unsigned oo = (64 * rg + 16 * (w & 3) + tok) * 1280 + ct * 160 + g4 * 8 + c0 * 32;
  u32x2 xres[3];
  xres[0] = *(const u32x2*)(x + oo); xres[1] = *(const u32x2*)(x + oo + 32);
  xres[2] = *(const u32x2*)(x + oo + (w < 4 ? 64 : 32));
  SB(); slot(6); SB();
  slot(7); SB();
  if (w < 4) slot(8);
  SB();
  STAMP(13);
  lds_wait(CNT3, 8);
#pragma unroll
  for (int r = 0; r < 4; ++r)
#pragma unroll
    for (int c = 0; c < 5; ++c)
      if (r * 5 + c < 19 || w != 7) *(LAS f32x4*)(size_t)((w ^ 4) * 20480 + (r * 5 + c) * 1024 + lg16) = acc[r][c];
  wait_lgkm<0>();
  barrier();
  STAMP(14);
#pragma unroll
  for (int c = 0; c < 3; ++c) {
    if (c < 2 || w < 4) {
      const unsigned a = ((w & 3) * 5 + c0 + c) * 1024 + lg16;
      f32x4 t3 = lds_rdf4(a + 61440);
      if (c == 1 && w == 7) t3 = acc[3][4];
      f32x4 t = ((lds_rdf4(a) + lds_rdf4(a + 20480)) + (lds_rdf4(a + 40960) + t3)) +
                ((lds_rdf4(a + 81920) + lds_rdf4(a + 102400)) + (lds_rdf4(a + 122880) + lds_rdf4(a + 143360)));
      u32x2 val = {pk2(t[0] + lo_f(xres[c][0]), t[1] + hi_f(xres[c][0])), pk2(t[2] + lo_f(xres[c][1]), t[3] + hi_f(xres[c][1]))};
      *(u32x2*)(out + oo + c * 32) = val;
    }
  }
  if (b == 0 && w == 7) st32(rws, fresh_lane() == 0 ? 0u : OOB, GEN_OFF, tag);
  STAMP(15);
  STAMP_FLUSH(0);
}

extern "C" void kernel_launch(void* const* d_in, const int* in_sizes, int n_in, void* d_out, int out_size,
                              void* d_ws, size_t ws_size, hipStream_t stream) {
  (void)hipFuncSetAttribute((const void*)fused, hipFuncAttributeMaxDynamicSharedMemorySize, 163840);
  hipLaunchKernelGGL(fused, dim3(256), dim3(512), 163840, stream, (const char*)d_in[0], (const char*)d_in[1],
                     (const char*)d_in[2], (const char*)d_in[4], (const char*)d_in[5], (const char*)d_in[6],
                     (char*)d_ws, (char*)d_out);
}
```

```cpp
#include <hip/hip_runtime.h>
#include <hip/hip_bf16.h>
#include <stdint.h>
#include <utility>

typedef __attribute__((ext_vector_type(2))) __bf16 bf16x2;
typedef __attribute__((ext_vector_type(4))) __bf16 bf16x4;
typedef __attribute__((ext_vector_type(8))) __bf16 bf16x8;
typedef __attribute__((ext_vector_type(4))) short s16x4;
typedef __attribute__((ext_vector_type(2))) float f32x2;
typedef __attribute__((ext_vector_type(4))) float f32x4;
typedef __attribute__((ext_vector_type(16))) float f32x16;
typedef __attribute__((ext_vector_type(2))) unsigned u32x2;
typedef __attribute__((ext_vector_type(4))) unsigned u32x4;
#define LAS __attribute__((address_space(3)))
#define DI __device__ __forceinline__
#define SB() __builtin_amdgcn_sched_barrier(0)
#define IC(v) std::integral_constant<int, (v)>{}

constexpr unsigned QKV_OFF = 0;
constexpr unsigned A2H_OFF = 8388608;
constexpr unsigned A2O_OFF = A2H_OFF + 6291456;
constexpr unsigned B2H_OFF = A2O_OFF + 2621440;
constexpr unsigned B2O_OFF = B2H_OFF + 1966080;
constexpr unsigned CS_OFF = B2O_OFF + 819200;
constexpr unsigned F0_OFF = CS_OFF + 409600;
constexpr unsigned F1_OFF = F0_OFF + 1024;
constexpr unsigned F2_OFF = F1_OFF + 8192;
constexpr unsigned FH_OFF = F2_OFF + 8192;
constexpr unsigned F3_OFF = FH_OFF + 2048;
constexpr unsigned GEN_OFF = F3_OFF + 4096;
constexpr unsigned GR_OFF = GEN_OFF + 1024;
constexpr unsigned WS_END = GR_OFF + 16384;
constexpr unsigned OOB = 0x80000000u;

#define STAMP_DECL
#define STAMP(S)
#define STAMP_FLUSH(K)

#define PSTAMP(S)
#define TSTAMP(S) STAMP(S)
constexpr float LOG2E = 1.4426950408889634f;
constexpr float QSCALE = 0.22360679774997896f * LOG2E;

template <int B, class F, int... I> DI void static_for_impl(F&& f, std::integer_sequence<int, I...>) { (f(std::integral_constant<int, B + I>{}), ...); }
template <int B, int E, class F> DI void static_rng(F&& f) { static_for_impl<B>(f, std::make_integer_sequence<int, E - B>{}); }
template <int N, class F> DI void static_for(F&& f) { static_rng<0, N>(f); }

template <int N> DI void wait_vm() { asm volatile("s_waitcnt vmcnt(%0)" :: "i"(N) : "memory"); }
template <int N> DI void wait_vm_l0() { asm volatile("s_waitcnt vmcnt(%0) lgkmcnt(0)" :: "i"(N) : "memory"); }
DI void wait_l0() { asm volatile("s_waitcnt lgkmcnt(0)" ::: "memory"); }
DI void barrier() { __builtin_amdgcn_s_barrier(); asm volatile("" ::: "memory"); }

template <unsigned LB> DI void dma_k(unsigned voff, u32x4 srd, unsigned k, unsigned step, unsigned wb) {
  unsigned t;
  asm volatile("s_add_u32 m0, %5, %6\n\ts_mul_i32 %0, %3, %4\n\tbuffer_load_dwordx4 %1, %2, %0 offen lds"
               : "=&s"(t) : "v"(voff), "s"(srd), "s"(k), "s"(step), "s"(wb), "i"(LB) : "memory", "scc");
}
template <unsigned LB> DI void dma_x(unsigned v0, unsigned v1, u32x4 srd, unsigned k, unsigned wb) {
  unsigned t;
  asm volatile("s_add_u32 m0, %5, %6\n\ts_lshl_b32 %0, %4, 7\n\tbuffer_load_dwordx4 %1, %3, %0 offen lds\n\t"
               "buffer_load_dwordx4 %2, %3, %0 offen offset:1024 lds"
               : "=&s"(t) : "v"(v0), "v"(v1), "s"(srd), "s"(k), "s"(wb), "i"(LB) : "memory", "scc");
}
template <int N> DI void dma_lin(unsigned voff, u32x4 srd, unsigned soff, unsigned lds) {
  static_assert(N >= 1 && N <= 4, "");
  if constexpr (N == 1)
    asm volatile("s_mov_b32 m0, %3\n\ts_nop 0\n\tbuffer_load_dwordx4 %0, %1, %2 offen sc1 lds" :: "v"(voff), "s"(srd), "s"(soff), "s"(lds) : "memory");
  if constexpr (N == 2)
    asm volatile("s_mov_b32 m0, %3\n\ts_nop 0\n\tbuffer_load_dwordx4 %0, %1, %2 offen sc1 lds\n\t"
                 "buffer_load_dwordx4 %0, %1, %2 offen offset:1024 sc1 lds" :: "v"(voff), "s"(srd), "s"(soff), "s"(lds) : "memory");
  if constexpr (N == 3)
    asm volatile("s_mov_b32 m0, %3\n\ts_nop 0\n\tbuffer_load_dwordx4 %0, %1, %2 offen sc1 lds\n\t"
                 "buffer_load_dwordx4 %0, %1, %2 offen offset:1024 sc1 lds\n\t"
                 "buffer_load_dwordx4 %0, %1, %2 offen offset:2048 sc1 lds" :: "v"(voff), "s"(srd), "s"(soff), "s"(lds) : "memory");
  if constexpr (N == 4)
    asm volatile("s_mov_b32 m0, %3\n\ts_nop 0\n\tbuffer_load_dwordx4 %0, %1, %2 offen sc1 lds\n\t"
                 "buffer_load_dwordx4 %0, %1, %2 offen offset:1024 sc1 lds\n\t"
                 "buffer_load_dwordx4 %0, %1, %2 offen offset:2048 sc1 lds\n\t"
                 "buffer_load_dwordx4 %0, %1, %2 offen offset:3072 sc1 lds" :: "v"(voff), "s"(srd), "s"(soff), "s"(lds) : "memory");
}
DI void dma_g(unsigned voff, const void* sbase, unsigned lds) {
  asm volatile("s_mov_b32 m0, %2\n\ts_nop 0\n\tglobal_load_lds_dwordx4 %0, %1" :: "v"(voff), "s"(sbase), "s"(lds) : "memory");
}

DI bf16x8 lds_rd128(unsigned a) { return *(LAS bf16x8*)(size_t)a; }
DI f32x4 lds_rdf4(unsigned a) { return *(LAS f32x4*)(size_t)a; }
DI bf16x4 lds_tr(unsigned a) { return __builtin_amdgcn_ds_read_tr16_b64_v4bf16((LAS bf16x4*)(size_t)a); }
DI bf16x8 cat8(bf16x4 lo, bf16x4 hi) { return __builtin_shufflevector(lo, hi, 0, 1, 2, 3, 4, 5, 6, 7); }
DI bf16x8 lds_trfrag(unsigned a, unsigned step) { return cat8(lds_tr(a), lds_tr(a + step)); }
DI unsigned pk2(float a, float b) {
  f32x2 v = {a, b};
  return __builtin_bit_cast(unsigned, __builtin_convertvector(v, bf16x2));
}
DI float bfr(float a) { return (float)(__bf16)a; }
DI float lo_f(unsigned u) { return __uint_as_float(u << 16); }
DI float hi_f(unsigned u) { return __uint_as_float(u & 0xffff0000u); }
DI bf16x8 bf8(u32x4 v) { return __builtin_bit_cast(bf16x8, v); }
DI f32x16 mfma32(bf16x8 a, bf16x8 b, f32x16 c) { return __builtin_amdgcn_mfma_f32_32x32x16_bf16(a, b, c, 0, 0, 0); }
DI f32x4 mfma16(bf16x8 a, bf16x8 b, f32x4 c) { return __builtin_amdgcn_mfma_f32_16x16x32_bf16(a, b, c, 0, 0, 0); }
DI f32x16 mfma8(u32x2 a, u32x2 b, f32x16 c) {
  return __builtin_amdgcn_mfma_f32_32x32x8bf16_1k(__builtin_bit_cast(s16x4, a), __builtin_bit_cast(s16x4, b), c, 0, 0, 0);
}
typedef __amdgpu_buffer_rsrc_t rsrc_t;
DI rsrc_t mkrsrc(const void* p, unsigned n) { return __builtin_amdgcn_make_buffer_rsrc((void*)p, 0, n, 0x00020000); }
DI void st128(rsrc_t r, unsigned vo, unsigned so, u32x4 v) {
  __builtin_amdgcn_raw_buffer_store_b128(v, r, vo, so, 16);
  asm volatile("s_nop 1" :: "v"(v) : "memory");
}
DI void st64(rsrc_t r, unsigned vo, unsigned so, u32x2 v) { __builtin_amdgcn_raw_buffer_store_b64(v, r, vo, so, 16); }
DI void st32(rsrc_t r, unsigned vo, unsigned so, unsigned v) { __builtin_amdgcn_raw_buffer_store_b32(v, r, vo, so, 16); }
DI u32x4 ld128(rsrc_t r, unsigned vo, unsigned so) { return __builtin_amdgcn_raw_buffer_load_b128(r, vo, so, 16); }
DI u32x2 ld64(rsrc_t r, unsigned vo, unsigned so) { return __builtin_amdgcn_raw_buffer_load_b64(r, vo, so, 16); }
DI unsigned ld32(rsrc_t r, unsigned vo, unsigned so) { return __builtin_amdgcn_raw_buffer_load_b32(r, vo, so, 16); }
DI u32x4 mkdesc(const void* p, unsigned n) {
  const unsigned long long a = (unsigned long long)p;
  return (u32x4){(unsigned)a, (unsigned)(a >> 32) & 0xffffu, n, 0x00020000u};
}
template <int CTRL> DI float dpp_add(float v) {
  return v + __uint_as_float(__builtin_amdgcn_update_dpp(0, __float_as_uint(v), CTRL, 0xf, 0xf, true));
}
DI float wave_sum(float v) {
  v = dpp_add<0x111>(v); v = dpp_add<0x112>(v); v = dpp_add<0x114>(v); v = dpp_add<0x118>(v);
  const float a = __uint_as_float(__builtin_amdgcn_readlane(__float_as_uint(v), 15));
  const float b = __uint_as_float(__builtin_amdgcn_readlane(__float_as_uint(v), 31));
  const float c = __uint_as_float(__builtin_amdgcn_readlane(__float_as_uint(v), 47));
  const float d = __uint_as_float(__builtin_amdgcn_readlane(__float_as_uint(v), 63));
  return (a + b) + (c + d);
}
DI int fresh_lane() {
  int l; asm volatile("v_mbcnt_lo_u32_b32 %0, -1, 0\n\tv_mbcnt_hi_u32_b32 %0, -1, %0" : "=v"(l)); return l;
}
DI int opq(int v) { asm volatile("" : "+s"(v)); return v; }
DI unsigned opq(unsigned v) { asm volatile("" : "+s"(v)); return v; }
DI unsigned vopq(unsigned v) { asm("" : "+v"(v)); return v; }
DI float vmul(float a, float b) { float r; asm("v_mul_f32 %0, %1, %2" : "=v"(r) : "v"(a), "v"(b)); return r; }
DI unsigned vadd(unsigned a, unsigned c) { unsigned r; asm volatile("v_add_u32 %0, %2, %1" : "=v"(r) : "v"(a), "s"(c)); return r; }
DI void lds_inc(unsigned a) {
  asm volatile("s_mov_b64 exec, 1\n\tds_add_u32 %0, %1\n\ts_mov_b64 exec, -1" :: "v"(a), "v"(1u) : "memory");
}
constexpr unsigned SPIN_CAP = 1u << 16;
DI void lds_wait(unsigned a, unsigned v) {
#pragma unroll 1
  for (unsigned sp = 0; sp < 64 * SPIN_CAP; ++sp) {
    if (__builtin_amdgcn_readfirstlane(*(volatile LAS unsigned*)(size_t)a) >= v) break;
    __builtin_amdgcn_s_sleep(1);
  }
  asm volatile("" ::: "memory");
}

constexpr unsigned XOFF = 98304, RTO = 73728, EX1 = 49152, EX2 = 51200;
constexpr unsigned xsl(int s) { return XOFF + (unsigned)(s & 3) * 16384u; }
constexpr unsigned wsl1(int s) { return (unsigned)(s & 3) * 24576u; }
constexpr unsigned wsl2(int u) {
  return u == 0 ? 57344u : u == 1 ? 73728u : u == 2 ? 0u : u == 3 ? 16384u : u == 4 ? 32768u :
         u == 5 ? 57344u : u == 6 ? 131072u : u == 7 ? 147456u : u == 8 ? 73728u : 98304u;
}
constexpr unsigned US = 8256, KO = 0, VO = 3 * US;
constexpr unsigned MISC = 61440, CNTA = MISC, CNTY = MISC + 4, CNTD = MISC + 8, RDYB = MISC + 16;
constexpr unsigned ONES = 55424, BO = MISC + 1024;
static_assert(6 * US <= EX2 && BO + 99 * 1024 == 163840 && (ONES & 255) == ((VO + 192) & 255) && ONES >= EX2 + 4096 && ONES + 5120 <= MISC, "lds");

constexpr int K_ST0 = 6, K_FL0 = 15, K_CS1 = 26, K_GR = 28, K_PL0 = 31, K_FL1 = 51, K_FH1 = 59;
constexpr int K_PL1 = 58, K_CS2 = 53, K_CH1 = 65, K_CH2 = 73, K_BH = 58, K_PH1 = 64, K_AH = 70;
struct Ops { short id[320]; short k[320]; short dma[320]; int n; };
constexpr int p2_at(int K) {
  return K == 29 ? 0 : K == 30 ? 1 : K == 31 ? 2 : K == 32 ? 3 : K == 35 ? 4 : K == 36 ? 5 : K == 39 ? 6 : K == 41 ? 7 :
         K == 43 ? 8 : K == 45 ? 9 : K == 47 ? 10 : K == 49 ? 11 : (K >= 50 && K <= 57) ? K - 38 : -1;
}
constexpr Ops make_ops(bool odd, bool y) {
  Ops o{}; int n = 0;
  auto add = [&](int id, int k, int d) { o.id[n] = (short)id; o.k[n] = (short)k; o.dma[n] = (short)d; ++n; };
  for (int i = 0; i < 5; ++i) add(100 + i, -3, 1);
  add(3, -3, 0); add(3, -3, 0); add(3, -3, 0);
  for (int i = 5; i < 10; ++i) add(100 + i, -3, 1);
  add(1, -3, 0); add(1, -3, 0);
  for (int i = 10; i < 15; ++i) add(100 + i, -3, 1);
  for (int K = 0; K < 64; ++K) {
    if (K >= 3 && K <= 30) {
      const int s = ((K - 3) >> 2) + 3, rel = (K - 3) & 3;
      if (rel == 0) { add(100 + 5 * s, K, 1); add(101 + 5 * s, K, 1); } else add(101 + 5 * s + rel, K, 1);
    }
    if (p2_at(K) >= 0) add(200 + p2_at(K), K, 1);
    if (K == K_ST0) for (int i = 0; i < 4; ++i) add(310, K, 0);
    if (K >= K_CS1 && K < K_CS1 + 5) add(325, K, 0);
    if (K == K_GR) add(320, K, 0);
    if (K == K_PL0) add(340, K, 0);
    if (K >= 40 && K < (odd ? 46 : 42)) add(330, K, 0);
    if (!odd && (K == 45 || K == 49)) add(331, K, 0);
    if (K == 44) add(332, K, 0);
    if (!y && K == K_PL1) add(350, K, 0);
    if (!y && K >= K_CS2 && K < K_CS2 + 5) add(350, K, 0);
    if (y && K >= K_BH) add(350, K, 0);
  }
  o.n = n; return o;
}
constexpr Ops OPS[4] = {make_ops(false, false), make_ops(true, false), make_ops(false, true), make_ops(true, true)};
constexpr bool stage_has(int id, int S) {
  return (id >= 100 && id < 200) ? (id - 100) / 5 <= S : (id >= 200 && id < 300) ? (id - 200) / 2 + 10 <= S : false;
}
constexpr int cmin(int a, int b) { return a < b ? a : b; }
constexpr int clampw(int w) { return w < 0 ? 0 : (w > 63 ? 63 : w); }
constexpr int wait_bar(const Ops& o, int K) {
  const int S = K == 63 ? 17 : (K >> 2) + 1;
  int issued = 0, last = -1;
  for (int i = 0; i < o.n; ++i) {
    if (o.k[i] < K) issued = i + 1;
    if (stage_has(o.id[i], S)) last = i;
    if (K == K_FL0 && o.id[i] == 310) last = last > i ? last : i;
    if (K == K_FL1 && o.id[i] == 330) last = last > i ? last : i;
    if (K == K_FH1 && o.id[i] == 331) last = last > i ? last : i;
  }
  return clampw(issued - 1 - last);
}
constexpr int wait_mid(const Ops& o, int K, int id) {
  int issued = 0, last = -1;
  for (int i = 0; i < o.n; ++i) { if (o.k[i] < K || (o.k[i] == K && o.dma[i])) issued = i + 1; if (o.id[i] == id) last = i; }
  return clampw(issued - 1 - last);
}

__global__ __launch_bounds__(512, 2) void fused(const char* __restrict__ x, const char* __restrict__ wqkv,
                                                const char* __restrict__ wo, const char* __restrict__ wg,
                                                const char* __restrict__ wu, const char* __restrict__ wd,
                                                char* __restrict__ ws, char* __restrict__ out) {
  const int b = blockIdx.x;
  const int mt = 8 * (b & 1) + (b >> 5), g = 4 * ((b >> 1) & 3) + ((b >> 3) & 3), odd = g & 1, p = g >> 1;
  const int w = __builtin_amdgcn_readfirstlane(threadIdx.x >> 6);
  const int rgp = w & 3, nlo = w >> 2;
  int lane = threadIdx.x & 63, h = lane >> 5, n = lane & 31;
  unsigned l16 = lane * 16;
#define REFRESH() do { lane = fresh_lane(); h = lane >> 5; n = lane & 31; l16 = lane * 16; } while (0)
  const int own = (mt & 7) + 2 * (mt >> 3);
  const int r0 = (own + ((0x8530 >> (4 * (g & 3))) & 15) + 2 * (g >> 2)) % 10;
  STAMP_DECL; STAMP(0);

  const unsigned wb1 = w * 1024, wb2 = w * 2048;
  const u32x4 sdx = mkdesc(x + (size_t)(128 * mt + 16 * w) * 1280, 0x40000000u);
  const u32x4 sdq = mkdesc(wqkv + (size_t)(8 * w) * 3840, 0x40000000u);
  const u32x4 sd1 = mkdesc(odd ? wqkv + (size_t)(8 * w) * 3840 : wg + (size_t)(8 * w) * 3072, 0x40000000u);
  const u32x4 sd2 = mkdesc(odd ? wqkv + (size_t)(8 * w) * 3840 : wu + (size_t)(8 * w) * 3072, 0x40000000u);
  const unsigned step12 = odd ? 245760u : 196608u;
  unsigned xv0, xv1, wv[3];
  {
    const int r = lane >> 3, u = lane & 7, sw = ((r >> 1) & 1) << 2;
    xv0 = r * 1280 + ((u ^ (r >> 1)) << 4);
    xv1 = (r + 8) * 1280 + ((u ^ (4 + (r >> 1))) << 4) - 1024;
    const unsigned vq = r * 3840 + ((u ^ sw) << 4);
    wv[0] = vq + (4 * p + odd) * 128;
    wv[1] = odd ? vq + (p == 7 ? 29 : 4 * p + 2) * 128 : r * 3072 + ((u ^ sw) << 4) + 3 * p * 128;
    wv[2] = odd ? vq + (p == 7 ? 29 : 4 * p + 3) * 128 : r * 3072 + ((u ^ sw ^ 2) << 4) + 3 * p * 128;
  }
  auto kst = [&](int pos) { int k = r0 + pos; return (unsigned)(k >= 10 ? k - 10 : k); };
  auto issue1 = [&](auto SS, auto RR) {
    constexpr int s = SS, rel = RR;
    if constexpr (rel == 0) dma_x<xsl(s)>(xv0, xv1, sdx, kst(s), wb2);
    else dma_k<wsl1(s) + (rel - 1) * 8192>(wv[rel - 1], rel == 1 ? sdq : rel == 2 ? sd1 : sd2, kst(s), rel == 1 ? 245760u : step12, wb1);
  };
  PSTAMP(7);
  static_for<4>([&](auto I) { issue1(IC(0), I); });
  SB();
  PSTAMP(8);
  barrier();
  STAMP(1);
  u32x4 sx4; unsigned sx1, gen;
  const int srow = 128 * mt + 8 * g + w;
  {
    const char* rp = x + (size_t)srow * 1280;
    asm volatile("global_load_dwordx4 %0, %1, %2" : "=v"(sx4) : "v"(lane * 16), "s"(rp) : "memory");
    asm volatile("global_load_dword %0, %1, %2 offset:1024" : "=v"(sx1) : "v"(lane * 4), "s"(rp) : "memory");
    asm volatile("global_load_dword %0, %1, %2 sc1" : "=v"(gen) : "v"(0), "s"(ws + GEN_OFF) : "memory");
  }
  static_for<4>([&](auto I) { issue1(IC(1), I); });
  const int jok = b < 170, jb = jok ? b : b - 170;
  const int jG = (jb * 241) >> 12, jks = (jb - 17 * jG) * 4 + (w >> 1);
  {
    const char* src = (jks < 48 ? wd + jks * (32 * 1280) : wo + (jks - 48) * (32 * 1280)) + (w & 1) * (16 * 1280) + jG * 128;
    const unsigned vo = (lane >> 3) * 1280 + (lane & 7) * 16;
    dma_g(vo, src, RTO + wb2);
    dma_g(vo, src + 8 * 1280, RTO + wb2 + 1024);
  }
  static_for<4>([&](auto I) { issue1(IC(2), I); });
  SB();
  PSTAMP(9);

  unsigned wa1[3], wa2[2], xa[4];
  {
    const int cb = (lane >> 4) & 1, q = (lane >> 2) & 3, pl = (lane >> 1) & 1, ph = lane & 1;
    const unsigned rowoff = (8 * h + q) * 128 + pl * 8;
    const int sw = (q >> 1) << 2;
#pragma unroll
    for (int i = 0; i < 3; ++i) {
      const int j = 3 * nlo + i;
      const int piece = odd ? (j >> 1) : 0, sel = odd ? (j & 1) : nlo, t = 2 * nlo + i - 1;
      const unsigned aq = piece * 8192 + (((4 * sel + 2 * cb + ph) ^ sw) << 4);
      const unsigned ag = (1 + cb) * 8192 + (((2 * t + ph) ^ sw ^ (cb << 1)) << 4);
      wa1[i] = rowoff + ((odd || i == 0) ? aq : ag);
    }
#pragma unroll
    for (int i = 0; i < 2; ++i) wa2[i] = rowoff + cb * 8192 + (((2 * (2 * nlo + i) + ph) ^ sw ^ (cb << 1)) << 4);
    const int row = 32 * rgp + n, sx = (row >> 1) & 7;
#pragma unroll
    for (int j = 0; j < 4; ++j) xa[j] = XOFF + row * 128 + (((2 * j + h) ^ sx) << 4);
  }
  auto mksel = [&]() {
    const int i = lane & 15, kg = lane >> 4;
    const unsigned o1 = ((i & 3) | (((i >> 2) ^ kg) & 1)) == 0 ? 0x3f803f80u : 0u;
    return bf8((u32x4){o1, o1, o1, o1});
  };
  bf16x8 selv = mksel();
  const rsrc_t rws = mkrsrc(ws, WS_END);
  const u32x4 sdw = mkdesc(ws, WS_END);
  SB();
  PSTAMP(10);
  wait_vm<15>();
  PSTAMP(14);
  barrier();
  STAMP(2);

  f32x16 acc1[3], acc2[2];
  f32x4 cs = (f32x4)(0.f);
  bf16x8 xr[56];
  bf16x8 wf[2][3], csf[2];
  unsigned wcs = rgp == 0 ? vopq(wa1[0]) : rgp == 1 ? vopq(wa1[1]) : vopq(wa1[2]);
  unsigned wcur[3];
  bf16x8 rt[2];
  u32x2 grn, cl[5];
  unsigned tag, nm;
  float smean;
  bf16x8 acs[3];
  float rstd, sdv, gsc;
  u32x4 pol, hq, sdg, sdu;
  unsigned pol1, st1 = 0, rdyh = 0, vs, vh, gv2, uv2;
  u32x4 q1[2]; u32x2 q2[2];
  u32x4 bq[12][5], aq[12][4];

  auto rd = [&](auto KK) {
    constexpr int K = KK;
    if constexpr (K < 40) {
      constexpr int s = K >> 2, j = K & 3;
      xr[K] = lds_rd128(xa[j] + (xsl(s) - XOFF));
      if constexpr (j == 0 && (s & 3) == 3) {
#pragma unroll
        for (int i = 0; i < 3; ++i) wcur[i] = vadd(wa1[i], wsl1(s));
      }
#pragma unroll
      for (int i = 0; i < 3; ++i) wf[K & 1][i] = lds_trfrag(((s & 3) == 3 ? wcur[i] : wa1[i] + wsl1(s)) + j * 2048, 512);
      if constexpr (K < 4) csf[K & 1] = lds_trfrag(wcs + wsl1(0) + j * 2048, 512);
    } else if constexpr (K < 80) {
      constexpr int u = (K - 40) >> 2, j = K & 3;
      if constexpr (u < 4) xr[K] = lds_rd128(xa[j] + (xsl(6 + u) - XOFF));
      constexpr bool far = wsl2(u) > 57344;
      if constexpr (j == 0 && far) {
#pragma unroll
        for (int i = 0; i < 2; ++i) wcur[i] = vadd(wa2[i], wsl2(u));
      }
#pragma unroll
      for (int i = 0; i < 2; ++i) wf[K & 1][i] = lds_trfrag((far ? wcur[i] : wa2[i] + wsl2(u)) + j * 2048, 512);
      if constexpr (K < 44) csf[K & 1] = lds_trfrag(wcs + wsl2(0) + j * 2048, 512);
    }
  };
  auto cs_ok = [&]() { return (((cl[0][1] ^ tag) | (cl[1][1] ^ tag)) | ((cl[2][1] ^ tag) | (cl[3][1] ^ tag)) | (cl[4][1] ^ tag)) == 0; };
  auto cs_fin = [&](unsigned src, unsigned dst) {
    if (!__all(cs_ok())) {
#pragma unroll 1
      for (unsigned sp = 0; sp < SPIN_CAP; ++sp) {
        __builtin_amdgcn_s_sleep(4);
#pragma unroll
        for (int t = 0; t < 5; ++t) cl[t] = ld64(rws, h * 1280 + n * 8 + t * 256, src);
        if (__all(cs_ok())) break;
      }
    }
    const float c = ((__uint_as_float(cl[0][0]) + __uint_as_float(cl[1][0])) + (__uint_as_float(cl[2][0]) + __uint_as_float(cl[3][0]))) + __uint_as_float(cl[4][0]);
    const unsigned u0 = __float_as_uint(c);
    const float r1 = c - __uint_as_float(u0 & 0xffff0000u);
    const unsigned u1 = __float_as_uint(r1);
    const float r2 = r1 - __uint_as_float(u1 & 0xffff0000u);
    *(LAS u32x4*)(size_t)(dst + l16) = (u32x4){__builtin_amdgcn_perm(u1, u0, 0x07060302u), __float_as_uint(r2) >> 16, 0u, 0u};
  };
  auto qkv_st = [&](const f32x16& a16, int cu0, int a) {
    const float r = rstd * (cu0 < 80 ? QSCALE : 1.f);
    u32x4 val;
#pragma unroll
    for (int i = 0; i < 4; ++i) val[i] = pk2(vmul(a16[8 * a + 2 * i], r), vmul(a16[8 * a + 2 * i + 1], r));
    st128(rws, vs, QKV_OFF + (cu0 + 2 * a) * 32768 + (128 * mt + 32 * rgp) * 16, val);
  };
  auto gu2 = [&](const f32x16& a16, int i) {
    float t0, t1; unsigned r;
    asm("v_mul_f32 %1, %3, %7\n\tv_mul_f32 %2, %4, %7\n\tv_exp_f32 %1, %1\n\tv_exp_f32 %2, %2\n\t"
        "v_fma_f32 %1, %1, %8, %8\n\tv_fma_f32 %2, %2, %8, %8\n\tv_rcp_f32 %1, %1\n\tv_rcp_f32 %2, %2\n\t"
        "v_mul_f32 %1, %5, %1\n\tv_mul_f32 %2, %6, %2\n\tv_cvt_pk_bf16_f32 %0, %1, %2"
        : "=v"(r), "=&v"(t0), "=&v"(t1)
        : "v"(a16[2 * i]), "v"(a16[2 * i + 1]), "v"(a16[8 + 2 * i]), "v"(a16[9 + 2 * i]), "v"(gsc), "v"(sdv));
    hq[i] = r;
  };
  auto gu_st = [&](int unit, int t) {
    st128(rws, vh, A2H_OFF + (((2 * mt + (rgp >> 1)) * 48 + 2 * unit + (t >> 1)) * 4 + 2 * (rgp & 1)) * 1024 + (t & 1) * 512, hq);
  };
  auto kv_piece = [&](int c) {
    const int bb = opq(b), pair = 2 * (bb & 7) + ((bb >> 4) & 1), e = (bb >> 3) & 1, QB = bb >> 5;
    int t0 = 256 * QB - 256 + 128 * rgp; t0 = t0 < 0 ? 0 : t0;
    if (c < 6) dma_lin<2>(l16, sdw, QKV_OFF + (80 + 80 * (c / 3) + 5 * pair + 2 * e + c % 3) * 32768 + t0 * 16, c * US + rgp * 2048);
    else {
      const unsigned so = QKV_OFF + 5 * pair * 32768 + (256 * QB + 64 * rgp + 32 * (c - 6)) * 16;
      q1[c - 6] = ld128(rws, vs, so + 3 * e * 32768);
      q2[c - 6] = ld64(rws, h ? OOB : n * 16, so + 2 * 32768 + 8 * e);
    }
  };
  auto pol1_ld = [&]() {
    const int bb = opq(b);
    const bool on = (lane < 24) & (lane >= (bb >= 32 ? 0 : 12));
    const unsigned v = ld32(rws, on ? lane * 4 : OOB, F1_OFF + ((bb & 7) * 8 + (bb >> 5)) * 128);
    return on ? v : tag;
  };
  unsigned av[4], l16b, kt1, kt2, abase, bbase, soa, sob;
  auto ksof = [&](int s) { return ((s < 4 ? opq(kt1) >> (4 * s) : opq(kt2) >> (4 * (s - 4))) & 15u); };
  auto ldB = [&](int s, int c) {
    if (c == 0) sob = bbase + ksof(s) * 5120;
    bq[s][c] = c < 4 ? ld128(rws, l16 + c * 1024, sob) : ld128(rws, l16b, sob);
  };
  auto ldA = [&](int s, int r) {
    if (r == 0) soa = abase + ksof(s) * 4096;
    aq[s][r] = ld128(rws, av[r], soa);
  };
  auto polh_ld = [&]() {
    const int bb = opq(b);
    return ld32(rws, (lane & 1) * 4, FH_OFF + (2 * (bb & 7) + (bb >> 7)) * 128 + rgp * 8);
  };

  auto step = [&](auto KK, auto OD, auto RL) {
    constexpr int K = KK, j = K & 3, ODD = OD, ROLE = RL;
    if constexpr (K == 40) STAMP(3);
    if constexpr (j == 3 && K <= 63) {
      constexpr int nx = ODD < 2 ? wait_bar(OPS[ODD & 1], K) : cmin(wait_bar(OPS[0], K), wait_bar(OPS[1], K));
      constexpr int ny = ODD < 2 ? wait_bar(OPS[2 + (ODD & 1)], K) : cmin(wait_bar(OPS[2], K), wait_bar(OPS[3], K));
      static_assert(ROLE < 2 || nx == ny, "");
      wait_vm_l0<(ROLE == 1 ? ny : nx)>();
      barrier();
      if constexpr (K == K_FL0) { if (w == 0) st32(rws, fresh_lane() == 0 ? 0u : OOB, F0_OFF + b * 4, tag); }
      if constexpr (K == K_FL1) {
        if (w == 0) {
          const int l = fresh_lane(), ui = l >> 2, hi = (l >> 1) & 1, qi = l & 1;
          const int u = 4 * p + odd + ui;
          const int G = ((u * 4 * 205) >> 10) + hi;
          const int fu = (5 * G) >> 2;
          const int qb = (mt >> 1) + qi;
          const bool on = (ui < (odd ? 3 : 1)) & (u < 30) & (fu <= u) & (qb < 8);
          const unsigned a = ((G & 7) * 8 + qb) * 128 + ((mt - 2 * qb + 2) * 6 + (G >> 3) * 2 + (u - fu)) * 4;
          st32(rws, on ? a : OOB, F1_OFF, tag);
        }
      }
      if constexpr (K == K_FH1 && ROLE == 0) { if (w == 0 && !odd) st32(rws, fresh_lane() == 0 ? 0u : OOB, FH_OFF + mt * 128 + p * 4, tag); }
    }
    if constexpr (K == 71) { wait_l0(); barrier(); }
    rd(IC(K + 1));
    if constexpr (K >= 3 && K <= 30) issue1(IC(((K - 3) >> 2) + 3), IC((K - 3) & 3));
    if constexpr (p2_at(K) >= 0) {
      constexpr int u = p2_at(K) >> 1, jj = p2_at(K) & 1;
      dma_k<wsl2(u) + jj * 8192>(jj ? uv2 : gv2, jj ? sdu : sdg, kst((u + 6) % 10), 196608u, wb1);
    }
    if constexpr (K < 40) {
#pragma unroll
      for (int i = 0; i < 3; ++i) acc1[i] = mfma32(wf[K & 1][i], xr[K], K == 0 ? (f32x16)(0.f) : acc1[i]);
      __builtin_amdgcn_sched_group_barrier(0x8, 1, 0); __builtin_amdgcn_sched_group_barrier(0x100, 3, 0);
      __builtin_amdgcn_sched_group_barrier(0x8, 1, 0); __builtin_amdgcn_sched_group_barrier(0x100, 2, 0);
      __builtin_amdgcn_sched_group_barrier(0x8, 1, 0); __builtin_amdgcn_sched_group_barrier(0x100, 2, 0);
    } else {
      constexpr int u = (K - 40) >> 2;
#pragma unroll
      for (int i = 0; i < 2; ++i) acc2[i] = mfma32(wf[K & 1][i], xr[u < 4 ? K : K - 56], K == 40 ? (f32x16)(0.f) : acc2[i]);
      __builtin_amdgcn_sched_group_barrier(0x8, 1, 0); __builtin_amdgcn_sched_group_barrier(0x100, 3, 0);
      __builtin_amdgcn_sched_group_barrier(0x8, 1, 0); __builtin_amdgcn_sched_group_barrier(0x100, 5, 0);
    }
    SB();
    if constexpr (K < 40) asm volatile("" :: "v"(acc1[0]), "v"(acc1[1]), "v"(acc1[2]));
    else asm volatile("" :: "v"(acc2[0]), "v"(acc2[1]));
    if constexpr (K < 4 || (K >= 40 && K < 44)) cs = mfma16(selv, csf[K & 1], cs);
    if constexpr (K == 0) {
      asm volatile("s_waitcnt vmcnt(%3)" : "+v"(sx4), "+v"(sx1), "+v"(gen) : "i"(wait_mid(OPS[0], K, 3)) : "memory");
      tag = __builtin_amdgcn_readfirstlane(gen) * 0x9E3779B1u + 0x7F4A7C15u;
      float s = lo_f(sx1) + hi_f(sx1);
#pragma unroll
      for (int i = 0; i < 4; ++i) s += lo_f(sx4[i]) + hi_f(sx4[i]);
      smean = wave_sum(s) * (1.f / 640.f);
    }
    if constexpr (K == 2) {
      wait_vm<wait_mid(OPS[0], K, 1)>();
      const unsigned a = RTO + wb2 + ((lane >> 2) & 3) * 128 + (lane >> 4) * 32 + (lane & 3) * 8;
      rt[0] = lds_trfrag(a, 512); rt[1] = lds_trfrag(a + 1024, 512);
    }
    if constexpr (K == 3) asm volatile("" : "+v"(rt[0]), "+v"(rt[1]) :: "memory");
    if constexpr (K == 4) {
      float q = 0.f;
#pragma unroll
      for (int i = 0; i < 5; ++i) {
        const unsigned u = i < 4 ? sx4[i] : sx1;
        const float d0 = lo_f(u) - smean, d1 = hi_f(u) - smean;
        q = __builtin_fmaf(d0, d0, q); q = __builtin_fmaf(d1, d1, q);
      }
      const float var = wave_sum(q) * (1.f / 640.f);
      grn[0] = pk2(smean, __builtin_amdgcn_sqrtf(bfr(bfr(var) + 1e-5f)));
      grn[1] = tag;
    }
    if constexpr (K == K_ST0 || K == 44) {
      REFRESH();
      const bool on = lane < ((rgp < (K == 44 ? 2 : 3) && (mt < 8 || mt >= 14)) ? 32 : 0);
      st64(rws, on ? n * 8 : OOB, CS_OFF + ((g * 10 + (K == 44 ? 6 + 2 * nlo : 3 * nlo) + rgp) * 10 + own) * 256,
           (u32x2){__float_as_uint(cs[0]), tag});
    }
    if constexpr (K == K_ST0) {
      st64(rws, lane == 0 ? 0u : OOB, GR_OFF + srow * 8, grn);
      const int f = 4 * jG + (lane >> 4), ct = (f * 205) >> 10, c = f - 5 * ct;
      const unsigned vo = jok ? (ct * (jks < 48 ? 240 : 100) + c) * 1024 + (lane & 15) * 16 : OOB;
      const unsigned so = (jks < 48 ? B2H_OFF + jks * 5120 : B2O_OFF + (jks - 48) * 5120) + (w & 1) * 512;
      st128(rws, vo, so, __builtin_bit_cast(u32x4, rt[0]));
      st128(rws, vo + 256, so, __builtin_bit_cast(u32x4, rt[1]));
    }
    if constexpr (K == 24) {
      REFRESH();
      const int r = lane >> 3, u = lane & 7, sw = ((r >> 1) & 1) << 2;
      gv2 = r * 3072 + ((u ^ sw) << 4) + (3 * p + 1 + odd) * 128;
      uv2 = r * 3072 + ((u ^ sw ^ 2) << 4) + (3 * p + 1 + odd) * 128;
    }
    if constexpr (K == 28) sdg = mkdesc(wg + (size_t)(8 * opq(w)) * 3072, 0x40000000u);
    if constexpr (K == 29) sdu = mkdesc(wu + (size_t)(8 * opq(w)) * 3072, 0x40000000u);
    if constexpr (K >= K_CS1 && K < K_CS1 + 5) {
      if constexpr (K == K_CS1) { REFRESH(); vs = w < 6 ? h * 1280 + n * 8 : OOB; }
      asm volatile("buffer_load_dwordx2 %0, %1, %2, %3 offen offset:%4 sc1" : "=v"(cl[K - K_CS1])
                   : "v"(vs), "s"(sdw), "s"(CS_OFF + (g * 10 + w) * 2560), "i"((K - K_CS1) * 256) : "memory");
    }
    if constexpr (K == K_GR)
      asm volatile("buffer_load_dwordx2 %0, %1, %2, %3 offen sc1" : "=v"(grn) : "v"(n * 8), "s"(sdw), "s"(GR_OFF + (128 * mt + 32 * rgp) * 8) : "memory");
    if constexpr (K == K_PL0)
      asm volatile("buffer_load_dwordx4 %0, %1, %2, %3 offen sc1" : "=v"(pol) : "v"(w == 7 ? l16 : OOB), "s"(sdw), "s"(F0_OFF) : "memory");
    if constexpr (K == 34) {
      asm volatile("s_waitcnt vmcnt(%6)" : "+v"(grn), "+v"(cl[0]), "+v"(cl[1]), "+v"(cl[2]), "+v"(cl[3]), "+v"(cl[4])
                   : "i"(wait_mid(OPS[0], K, 325)) : "memory");
      if (w < 6) cs_fin(CS_OFF + (g * 10 + w) * 2560, EX1 + wb1);
      if (!__all(grn[1] == tag)) {
#pragma unroll 1
        for (unsigned sp = 0; sp < SPIN_CAP; ++sp) {
          __builtin_amdgcn_s_sleep(4);
          grn = ld64(rws, n * 8, GR_OFF + (128 * mt + 32 * rgp) * 8);
          if (__all(grn[1] == tag)) break;
        }
      }
      nm = ((grn[0] & 0xffffu) ^ 0x8000u) * 0x10001u;
      sdv = hi_f(grn[0]);
      rstd = __builtin_amdgcn_rcpf(sdv);
      gsc = -LOG2E * rstd;
    }
    if constexpr (K == 35) {
#pragma unroll
      for (int i = 0; i < 3; ++i) acs[i] = lds_rd128(EX1 + (3 * nlo + i) * 1024 + l16);
    }
    if constexpr (K >= 36 && K < 39) acc1[K - 36] = mfma32(acs[K - 36], bf8((u32x4){nm, nm, nm, nm}), acc1[K - 36]);
    if constexpr (K == 38) {
      wcs = (rgp & 1) ? vopq(wa2[1]) : vopq(wa2[0]);
      asm volatile("s_waitcnt vmcnt(%1)" : "+v"(pol) : "i"(wait_mid(OPS[0], K, 340)) : "memory");
      if (w == 7) {
        auto ok = [&]() { return ((pol[0] ^ tag) | (pol[1] ^ tag) | (pol[2] ^ tag) | (pol[3] ^ tag)) == 0; };
        if (!__all(ok())) {
#pragma unroll 1
          for (unsigned sp = 0; sp < SPIN_CAP; ++sp) {
            __builtin_amdgcn_s_sleep(4);
            pol = ld128(rws, l16, F0_OFF);
            if (__all(ok())) break;
          }
        }
        if (b == 0) st32(rws, lane == 0 ? 0u : OOB, GEN_OFF, tag);
      }
    }
    if constexpr (K == 39) {
      REFRESH();
      vs = h * 32768 + n * 16;
      vh = (n >> 4) * 1024 + h * 256 + (n & 15) * 16;
      selv = mksel();
      cs = (f32x4)(0.f);
    }
    if constexpr (K == 40 || K == 41) qkv_st(acc1[0], odd ? 32 * p + 8 + 12 * nlo : 32 * p + 4 * nlo, K & 1);
    if constexpr (K >= 42 && K < 50) {
      constexpr int z = K - 42;
      if (odd) {
        if constexpr (K < 46) qkv_st(acc1[(K - 40) >> 1], 32 * p + 8 + 12 * nlo + 4 * ((K - 40) >> 1), K & 1);
      } else {
        gu2(acc1[1 + (z >> 2)], z & 3);
        if constexpr ((z & 3) == 3) gu_st(3 * p, 2 * nlo + (z >> 2));
      }
    }
    if constexpr (K == K_CS2 - 1) {
      REFRESH();
      if constexpr (ROLE == 1) {
#pragma unroll
        for (int r = 0; r < 4; ++r) av[r] = l16 + ((r + rgp) & 3) * 1024;
        l16b = l16 + 4096;
        const int bb = opq(b), ct = (bb >> 3) & 7;
        kt1 = 0x76107610u >> (4 * (ct & 3));
        kt2 = (unsigned)(0xBA985432BA985432ull >> (4 * ((ct + 2 * (bb >> 6)) & 7)));
        abase = A2H_OFF + ((4 * (bb & 7) + (bb >> 6)) * 48 + 12 * rgp) * 4096;
        bbase = B2H_OFF + (ct * 48 + 12 * rgp) * 5120;
      }
    }
    if constexpr (K >= K_CS2) {
      if constexpr (ROLE == 0) {
        if constexpr (K < K_CS2 + 5) cl[K - K_CS2] = ld64(rws, h * 1280 + n * 8 + (K - K_CS2) * 256, CS_OFF + (g * 10 + 6 + w) * 2560);
        if constexpr (K == K_PL1) pol1 = pol1_ld();
        if constexpr (K == K_CH1) {
          st1 = __all(pol1 == tag) ? 1 : 0;
          cs_fin(CS_OFF + (g * 10 + 6 + w) * 2560, EX2 + wb1);
        }
        if constexpr (K == K_CH1 + 1) { if (st1 == 0) pol1 = pol1_ld(); }
        if constexpr (K == K_CH2) { if (st1 == 0 && __all(pol1 == tag)) st1 = 2; }
        if constexpr (K >= K_CH1 && K < K_CH1 + 8) { if (st1 == 1) kv_piece(K - K_CH1); }
        if constexpr (K >= K_CH2 && K < K_CH2 + 7) { if (st1 == 2) { kv_piece(K - K_CH2); if constexpr (K == K_CH2 + 6) kv_piece(7); } }
      } else {
        if constexpr (K >= K_BH && K < K_BH + 20) ldB((K - K_BH) / 5, (K - K_BH) % 5);
        if constexpr (K == K_PH1) pol1 = polh_ld();
        if constexpr (K == K_AH) rdyh = __all(pol1 == tag) ? 1 : 0;
        if constexpr (K >= K_AH && K < K_AH + 8) { if (rdyh) { ldA((K - K_AH) >> 1, 2 * (K & 1)); ldA((K - K_AH) >> 1, 2 * (K & 1) + 1); } }
      }
    }
    if constexpr (K == 71) {
#pragma unroll
      for (int i = 0; i < 2; ++i) acs[i] = lds_rd128(EX2 + (2 * nlo + i) * 1024 + l16);
    }
    if constexpr (K == 72 || K == 73) acc2[K - 72] = mfma32(acs[K - 72], bf8((u32x4){nm, nm, nm, nm}), acc2[K - 72]);
    if constexpr (K == 74) {
      const unsigned o1 = w < 5 ? 0x3f803f80u : 0u;
      if (w < 6) *(LAS u32x4*)(size_t)((w < 5 ? ONES + wb1 : MISC) + l16) = (u32x4){o1, o1, o1, o1};
    }
    SB();
  };
  rd(IC(0));
  static_rng<0, 52>([&](auto KK) { step(KK, IC(2), IC(2)); });

  auto epi2 = [&]() {
    STAMP(4);
    asm volatile("s_nop 7\n\ts_nop 7" : "+v"(acc2[0]), "+v"(acc2[1]));
#pragma unroll
    for (int i = 0; i < 2; ++i) {
#pragma unroll
      for (int z = 0; z < 4; ++z) gu2(acc2[i], z);
      gu_st(3 * p + 1 + odd, 2 * nlo + i);
    }
    SB();
  };
#define TAIL_COORDS \
  REFRESH(); \
  const int bb = opq(b), ct = (bb >> 3) & 7, rg = 4 * (bb & 7) + (bb >> 6); \
  const unsigned f2w = F2_OFF + (2 * mt + (rgp >> 1)) * 256 + (g * 4 + (rgp & 1) * 2 + nlo) * 4

  if (nlo == 0) {
    static_rng<52, 80>([&](auto KK) { step(KK, IC(2), IC(0)); });
    epi2();
    if (st1 == 0) {
#pragma unroll 1
      for (unsigned sp = 0; sp < SPIN_CAP; ++sp) {
        pol1 = pol1_ld();
        if (__all(pol1 == tag)) break;
        __builtin_amdgcn_s_sleep(4);
      }
      static_for<8>([&](auto I) { kv_piece(I); });
      wait_vm<0>();
    } else wait_vm<2>();
    wait_l0();
    barrier();
    STAMP(5);
    __builtin_amdgcn_s_setprio(3);
    TAIL_COORDS;
    const int e = (bb >> 3) & 1, QB = bb >> 5, pair = 2 * (bb & 7) + ((bb >> 4) & 1);
    const int jmin = QB ? 0 : 8 - 2 * rgp;
    const unsigned ka1 = KO + (e + h) * US + (64 * rgp + n) * 16;
    const unsigned ka2 = KO + (e ? 8 : 2 * US) + (64 * rgp + n) * 16;
    unsigned va;
    {
      const int g1 = (lane >> 4) & 1, q = (lane >> 2) & 3, pp = lane & 3;
      const unsigned um = (e + (pp & 1)) * US + (pp >> 1) * 8, ut = e ? 8 : 2 * US;
      unsigned uo = VO + (g1 ? ut : um) + rgp * 1024;
      asm volatile("" : "+v"(uo));
      va = ((g1 * pp) ? ONES : uo) + (4 * h + q) * 16;
    }
    auto rdK1 = [&](int j) { return lds_rd128(ka1 + j * 512); };
    auto rdK2 = [&](int j) { return *(LAS u32x2*)(size_t)(ka2 + j * 512); };
    auto rdV = [&](int j, int sh) { return lds_trfrag(va + j * 512 + sh * 256, 128); };
    bf16x8 k1[2], vf[2][2]; u32x2 k2[2];
    k1[1] = rdK1(9); k2[1] = rdK2(9); k1[0] = rdK1(8); k2[0] = rdK2(8);
    vf[1][0] = rdV(9, 0); vf[1][1] = rdV(9, 1);
    f32x16 cc[2], o[2], sc[2][2];
#pragma unroll
    for (int a = 1; a >= 0; --a) {
      const u32x4 ku = __builtin_bit_cast(u32x4, k1[a]);
      float t = 0.f;
#pragma unroll
      for (int i = 0; i < 6; ++i)
        t = __builtin_amdgcn_fdot2_f32_bf16(__builtin_bit_cast(bf16x2, i < 4 ? ku[i] : k2[a][i - 4]),
                                            __builtin_bit_cast(bf16x2, i < 4 ? q1[a][i] : q2[a][i - 4]), t, false);
      auto r = __builtin_amdgcn_permlane32_swap(__float_as_uint(t), __float_as_uint(t), false, false);
      cc[a] = (f32x16)(-(__uint_as_float(r[0]) + __uint_as_float(r[1])));
      o[a] = (f32x16)(0.f);
    }
    sc[1][1] = mfma8(k2[1], q2[1], mfma32(k1[1], bf8(q1[1]), cc[1]));
    unsigned mk[8];
#pragma unroll
    for (int r = 0; r < 8; ++r) {
      const int c = n - 4 * h - ((2 * r & 3) + 8 * (r >> 1));
      mk[r] = (c >= 0 ? 0xffffu : 0u) | (c >= 1 ? 0xffff0000u : 0u);
    }
    SB();
    auto expk = [&](const f32x16& s, int sh, int mode) {
      u32x4 pu;
#pragma unroll
      for (int r = 0; r < 4; ++r) {
        pu[r] = pk2(__builtin_amdgcn_exp2f(s[8 * sh + 2 * r]), __builtin_amdgcn_exp2f(s[8 * sh + 2 * r + 1]));
        if (mode == 1) pu[r] &= mk[4 * sh + r];
        if (mode == 2) pu[r] &= ~mk[4 * sh + r];
      }
      return bf8(pu);
    };
    static_for<10>([&](auto TT) {
      constexpr int J = 9 - TT, cu = J & 1, nx = cu ^ 1;
      constexpr bool hasA = J <= 8, hasB = J >= 1, nA = J >= 1, nB = J >= 2;
      constexpr int mA = J == 8 ? 1 : J == 0 ? 2 : 0, mB = J == 9 ? 1 : J == 1 ? 2 : 0;
      if (J >= jmin) {
        bf16x8 pp;
        if constexpr (nB) sc[nx][1] = mfma32(k1[nx], bf8(q1[1]), cc[1]);
        if constexpr (hasA) pp = expk(sc[cu][0], 0, mA);
        if constexpr (nA) sc[nx][0] = mfma32(k1[nx], bf8(q1[0]), cc[0]);
        if constexpr (J >= 2) k1[cu] = rdK1(J - 2);
        if constexpr (J >= 1) vf[nx][0] = rdV(J - 1, 0);
        SB();
        if constexpr (hasA) { o[0] = mfma32(vf[cu][0], pp, o[0]); pp = expk(sc[cu][0], 1, mA); }
        if constexpr (nB) sc[nx][1] = mfma8(k2[nx], q2[1], sc[nx][1]);
        if constexpr (J >= 1) vf[nx][1] = rdV(J - 1, 1);
        SB();
        if constexpr (hasA) o[0] = mfma32(vf[cu][1], pp, o[0]);
        if constexpr (hasB) pp = expk(sc[cu][1], 0, mB);
        if constexpr (nA) sc[nx][0] = mfma8(k2[nx], q2[0], sc[nx][0]);
        if constexpr (J >= 2) k2[cu] = rdK2(J - 2);
        SB();
        if constexpr (hasB) {
          o[1] = mfma32(vf[cu][0], pp, o[1]); pp = expk(sc[cu][1], 1, mB);
          o[1] = mfma32(vf[cu][1], pp, o[1]);
        }
        SB();
      }
      if constexpr (J == 8) {
        wait_vm<0>();
        st32(rws, lane == 0 ? 0u : OOB, f2w, tag);
        SB();
      }
    });
    STAMP(6);
#pragma unroll
    for (int a = 0; a < 2; ++a) {
      float amax = 0.f;
#pragma unroll
      for (int r = 0; r < 12; ++r) amax = __builtin_fmaxf(amax, __builtin_fabsf(o[a][r]));
      const bool okv = (o[a][12] >= 9.0e-13f) & (o[a][12] <= 1.0e12f) & (amax <= 3.0e38f);
      if (!__all(okv)) {
        float mx = -__builtin_inff();
#pragma unroll 1
        for (int pass = 0; pass < 2; ++pass) {
          o[a] = (f32x16)(0.f);
#pragma unroll 1
          for (int j = (jmin > a ? jmin : a); j < 9 + a; ++j) {
            f32x16 s = mfma8(rdK2(j), q2[a], mfma32(rdK1(j), bf8(q1[a]), (f32x16)(0.f)));
            const unsigned fl = j == a ? ~0u : 0u, al = (j == a || j == 8 + a) ? 0u : ~0u;
#pragma unroll
            for (int r = 0; r < 8; ++r) {
              const int m = (int)((mk[r] ^ fl) | al);
              s[2 * r] = (m & 1) ? s[2 * r] : -__builtin_inff();
              s[2 * r + 1] = m < 0 ? s[2 * r + 1] : -__builtin_inff();
            }
            if (pass == 0) {
#pragma unroll
              for (int r = 0; r < 16; ++r) mx = __builtin_fmaxf(mx, s[r]);
            } else {
#pragma unroll
              for (int r = 0; r < 16; ++r) s[r] -= mx;
              o[a] = mfma32(rdV(j, 0), expk(s, 0, 0), o[a]);
              o[a] = mfma32(rdV(j, 1), expk(s, 1, 0), o[a]);
            }
          }
          if (pass == 0) {
            auto t2 = __builtin_amdgcn_permlane32_swap(__float_as_uint(mx), __float_as_uint(mx), false, false);
            mx = __builtin_fmaxf(__uint_as_float(t2[0]), __uint_as_float(t2[1]));
          }
        }
      }
      const float il = __builtin_amdgcn_rcpf(o[a][12]);
      const unsigned so = A2O_OFF + (16 * QB + 4 * rgp + 2 * a) * 20480 + 5 * pair * 256;
      const unsigned vb = (n >> 4) * 20480 + (n & 15) * 16;
      st128(rws, vb + h * 256, so + 3 * e * 256,
            (u32x4){pk2(o[a][0] * il, o[a][1] * il), pk2(o[a][2] * il, o[a][3] * il), pk2(o[a][4] * il, o[a][5] * il), pk2(o[a][6] * il, o[a][7] * il)});
      st64(rws, h ? OOB : vb, so + 512 + 8 * e, (u32x2){pk2(o[a][8] * il, o[a][9] * il), pk2(o[a][10] * il, o[a][11] * il)});
    }
    SB();
    lds_inc(CNTA);
    const u32x4 blast = ld128(rws, l16, B2O_OFF + ct * 102400 + 99 * 1024);
    wait_vm<1>();
    st32(rws, lane == 0 ? 0u : OOB, F3_OFF + (4 * QB + rgp) * 128 + (2 * pair + e) * 4, tag);
    TSTAMP(7);
    unsigned p3 = ld32(rws, n * 4, F3_OFF + rg * 128);
    lds_wait(CNTD, 4);
    TSTAMP(8);
    bf16x8 bo[4][25];
    unsigned ba[4];
#pragma unroll
    for (int P = 0; P < 4; ++P) ba[P] = BO + ((P + ct) & 3) * 25600 + l16;
    auto rdB = [&](int P, int i) {
      bo[P][i] = lds_rd128(ba[P] + i * 1024);
      if (i == 24) bo[P][i] = ((P + ct) & 3) == 3 ? bf8(blast) : bo[P][i];
    };
    static_for<25>([&](auto I) { rdB(0, I); });
    SB();
    if (!__all(p3 == tag)) {
#pragma unroll 1
      for (unsigned sp = 0; sp < SPIN_CAP; ++sp) {
        p3 = ld32(rws, n * 4, F3_OFF + rg * 128);
        if (__all(p3 == tag)) break;
        __builtin_amdgcn_s_sleep(2);
      }
    }
    SB();
    TSTAMP(9);
    u32x4 ao[4][5];
#pragma unroll
    for (int P = 0; P < 4; ++P) {
      const unsigned so = A2O_OFF + (4 * rg + rgp) * 20480 + ((P + ct) & 3) * 5120;
#pragma unroll
      for (int i = 0; i < 5; ++i) ao[P][i] = i < 4 ? ld128(rws, l16 + i * 1024, so) : ld128(rws, l16, so + 4096);
    }
    SB();
    f32x4 acc[5];
    static_for<20>([&](auto II) {
      constexpr int P = II / 5, i = II % 5;
      if constexpr (P < 3) static_for<5>([&](auto C) { rdB(P + 1, 5 * i + C); });
#pragma unroll
      for (int c = 0; c < 5; ++c) acc[c] = mfma16(bo[P][5 * i + c], bf8(ao[P][i]), II == 0 ? (f32x4)(0.f) : acc[c]);
      SB();
    });
    TSTAMP(10);
    const unsigned oo = (64 * rg + 16 * rgp + (lane & 15)) * 1280 + ct * 160 + (lane >> 4) * 8;
    lds_wait(RDYB + rgp * 4, 1);
#pragma unroll
    for (int c = 0; c < 5; ++c) {
      const f32x4 t = acc[c] + lds_rdf4(rgp * 15360 + c * 1024 + l16);
      *(u32x2*)(out + oo + c * 32) = (u32x2){pk2(t[0], t[1]), pk2(t[2], t[3])};
    }
    STAMP(15);
    STAMP_FLUSH(0);
    __builtin_amdgcn_endpgm();
  } else {
    static_rng<52, 80>([&](auto KK) { step(KK, IC(2), IC(1)); });
    epi2();
    wait_l0();
    barrier();
    STAMP(5);
    TAIL_COORDS;
    if (!rdyh) {
#pragma unroll 1
      for (unsigned sp = 0; sp < SPIN_CAP; ++sp) {
        pol1 = polh_ld();
        if (__all(pol1 == tag)) break;
        __builtin_amdgcn_s_sleep(4);
      }
      static_for<16>([&](auto I) { ldA(I / 4, I % 4); });
    }
    asm volatile("" :: "v"(aq[3][3]), "v"(aq[3][2]), "v"(aq[3][1]), "v"(aq[3][0]), "v"(bq[3][4]));
    SB();
    f32x4 acc[4][5];
    auto slot = [&](int s) {
#pragma unroll
      for (int r = 0; r < 4; ++r)
#pragma unroll
        for (int c = 0; c < 5; ++c) acc[r][c] = mfma16(bf8(bq[s][c]), bf8(aq[s][r]), s == 0 ? (f32x4)(0.f) : acc[r][c]);
    };
    const unsigned bsrc = B2O_OFF + ct * 102400 + rgp * 25600, bdst = BO + rgp * 25600;
    auto dmaB = [&](int i) {
      if (i < 6) dma_lin<4>(l16, sdw, bsrc + i * 4096, bdst + i * 4096);
      else dma_lin<1>(l16, sdw, bsrc + (rgp < 3 ? 24576 : 23552), bdst + (rgp < 3 ? 24576 : 23552));
    };
    dmaB(0); dmaB(1); SB(); slot(0); SB();
    static_for<5>([&](auto C) { ldB(4, C); });
    dmaB(2); dmaB(3); SB(); slot(1); SB();
    wait_vm<21>();
    st32(rws, lane == 0 ? 0u : OOB, f2w, tag);
    static_for<5>([&](auto C) { ldB(5, C); });
    dmaB(4); dmaB(5); dmaB(6); SB(); slot(2); SB();
    static_for<10>([&](auto C) { ldB(6 + C / 5, C % 5); });
    SB(); slot(3); SB();
    STAMP(6);
    unsigned p2 = ld32(rws, (lane & 15) * 4, F2_OFF + rg * 256 + rgp * 64);
    wait_vm<11>();
    lds_inc(CNTD);
    SB();
    if (!__all(p2 == tag)) {
#pragma unroll 1
      for (unsigned sp = 0; sp < SPIN_CAP; ++sp) {
        p2 = ld32(rws, (lane & 15) * 4, F2_OFF + rg * 256 + rgp * 64);
        if (__all(p2 == tag)) break;
        __builtin_amdgcn_s_sleep(2);
      }
    }
    SB();
    TSTAMP(7);
    static_for<12>([&](auto I) { ldA(4 + I / 4, I % 4); });
    SB();
    u32x2 xres[5];
    static_rng<4, 12>([&](auto SS) {
      constexpr int s = SS;
      slot(s); SB();
      if constexpr (s + 3 < 12) static_for<4>([&](auto I) { ldA(s + 3, I); });
      if constexpr (s + 4 < 12) static_for<5>([&](auto C) { ldB(s + 4, C); });
      if constexpr (s == 9) {
        const unsigned oo = (64 * rg + 16 * rgp + (lane & 15)) * 1280 + ct * 160 + (lane >> 4) * 8;
#pragma unroll
        for (int c = 0; c < 5; ++c) xres[c] = *(const u32x2*)(x + oo + c * 32);
      }
      SB();
    });
    TSTAMP(8);
    lds_wait(CNTA, 4);
#pragma unroll
    for (int d = 1; d < 4; ++d) {
      const unsigned a = ((rgp + d) & 3) * 15360 + (d - 1) * 5120 + l16;
#pragma unroll
      for (int c = 0; c < 5; ++c) *(LAS f32x4*)(size_t)(a + c * 1024) = acc[d][c];
    }
    lds_inc(CNTY);
    lds_wait(CNTY, 4);
    TSTAMP(9);
    const unsigned a = rgp * 15360 + l16;
    f32x4 t[5];
#pragma unroll
    for (int c = 0; c < 5; ++c) {
      t[c] = (acc[0][c] + lds_rdf4(a + c * 1024)) + (lds_rdf4(a + 5120 + c * 1024) + lds_rdf4(a + 10240 + c * 1024));
      t[c] += (f32x4){lo_f(xres[c][0]), hi_f(xres[c][0]), lo_f(xres[c][1]), hi_f(xres[c][1])};
    }
    SB();
#pragma unroll
    for (int c = 0; c < 5; ++c) *(LAS f32x4*)(size_t)(a + c * 1024) = t[c];
    asm volatile("" ::: "memory");
    *(volatile LAS unsigned*)(size_t)(RDYB + rgp * 4) = 1u;
    TSTAMP(10);
  }
  STAMP(15);
  STAMP_FLUSH(0);
}

extern "C" void kernel_launch(void* const* d_in, const int* in_sizes, int n_in, void* d_out, int out_size,
                              void* d_ws, size_t ws_size, hipStream_t stream) {
  (void)hipFuncSetAttribute((const void*)fused, hipFuncAttributeMaxDynamicSharedMemorySize, 163840);
  hipLaunchKernelGGL(fused, dim3(256), dim3(512), 163840, stream, (const char*)d_in[0], (const char*)d_in[1],
                     (const char*)d_in[2], (const char*)d_in[4], (const char*)d_in[5], (const char*)d_in[6],
                     (char*)d_ws, (char*)d_out);
}
```

```cpp
#include <hip/hip_runtime.h>
#include <hip/hip_bf16.h>
#include <stdint.h>
#include <utility>

typedef __attribute__((ext_vector_type(2))) __bf16 bf16x2;
typedef __attribute__((ext_vector_type(4))) __bf16 bf16x4;
typedef __attribute__((ext_vector_type(8))) __bf16 bf16x8;
typedef __attribute__((ext_vector_type(4))) short s16x4;
typedef __attribute__((ext_vector_type(2))) float f32x2;
typedef __attribute__((ext_vector_type(4))) float f32x4;
typedef __attribute__((ext_vector_type(16))) float f32x16;
typedef __attribute__((ext_vector_type(2))) unsigned u32x2;
typedef __attribute__((ext_vector_type(4))) unsigned u32x4;
#define LAS __attribute__((address_space(3)))
#define DI __device__ __forceinline__
#define SB() __builtin_amdgcn_sched_barrier(0)
#define IC(v) std::integral_constant<int, (v)>{}

constexpr unsigned QKV_OFF = 0;
constexpr unsigned A2H_OFF = 8388608;
constexpr unsigned A2O_OFF = A2H_OFF + 6291456;
constexpr unsigned B2H_OFF = A2O_OFF + 2621440;
constexpr unsigned B2O_OFF = B2H_OFF + 1966080;
constexpr unsigned CS_OFF = B2O_OFF + 819200;
constexpr unsigned F0_OFF = CS_OFF + 409600;
constexpr unsigned F1_OFF = F0_OFF + 1024;
constexpr unsigned F2_OFF = F1_OFF + 8192;
constexpr unsigned FH_OFF = F2_OFF + 8192;
constexpr unsigned F3_OFF = FH_OFF + 2048;
constexpr unsigned GEN_OFF = F3_OFF + 4096;
constexpr unsigned GR_OFF = GEN_OFF + 1024;
constexpr unsigned WS_END = GR_OFF + 16384;
constexpr unsigned OOB = 0x80000000u;

#define STAMP_DECL
#define STAMP(S)
#define STAMP_FLUSH(K)

#define PSTAMP(S)
#define TSTAMP(S) STAMP(S)
constexpr float LOG2E = 1.4426950408889634f;
constexpr float QSCALE = 0.22360679774997896f * LOG2E;

template <int B, class F, int... I> DI void static_for_impl(F&& f, std::integer_sequence<int, I...>) { (f(std::integral_constant<int, B + I>{}), ...); }
template <int B, int E, class F> DI void static_rng(F&& f) { static_for_impl<B>(f, std::make_integer_sequence<int, E - B>{}); }
template <int N, class F> DI void static_for(F&& f) { static_rng<0, N>(f); }

template <int N> DI void wait_vm() { asm volatile("s_waitcnt vmcnt(%0)" :: "i"(N) : "memory"); }
template <int N> DI void wait_vm_l0() { asm volatile("s_waitcnt vmcnt(%0) lgkmcnt(0)" :: "i"(N) : "memory"); }
DI void wait_l0() { asm volatile("s_waitcnt lgkmcnt(0)" ::: "memory"); }
DI void barrier() { __builtin_amdgcn_s_barrier(); asm volatile("" ::: "memory"); }

template <unsigned LB> DI void dma_k(unsigned voff, u32x4 srd, unsigned k, unsigned step, unsigned wb) {
  unsigned t;
  asm volatile("s_add_u32 m0, %5, %6\n\ts_mul_i32 %0, %3, %4\n\tbuffer_load_dwordx4 %1, %2, %0 offen lds"
               : "=&s"(t) : "v"(voff), "s"(srd), "s"(k), "s"(step), "s"(wb), "i"(LB) : "memory", "scc");
}
template <unsigned LB> DI void dma_x(unsigned v0, unsigned v1, u32x4 srd, unsigned k, unsigned wb) {
  unsigned t;
  asm volatile("s_add_u32 m0, %5, %6\n\ts_lshl_b32 %0, %4, 7\n\tbuffer_load_dwordx4 %1, %3, %0 offen lds\n\t"
               "buffer_load_dwordx4 %2, %3, %0 offen offset:1024 lds"
               : "=&s"(t) : "v"(v0), "v"(v1), "s"(srd), "s"(k), "s"(wb), "i"(LB) : "memory", "scc");
}
template <int N> DI void dma_lin(unsigned voff, u32x4 srd, unsigned soff, unsigned lds) {
  static_assert(N >= 1 && N <= 4, "");
  if constexpr (N == 1)
    asm volatile("s_mov_b32 m0, %3\n\ts_nop 0\n\tbuffer_load_dwordx4 %0, %1, %2 offen sc1 lds" :: "v"(voff), "s"(srd), "s"(soff), "s"(lds) : "memory");
  if constexpr (N == 2)
    asm volatile("s_mov_b32 m0, %3\n\ts_nop 0\n\tbuffer_load_dwordx4 %0, %1, %2 offen sc1 lds\n\t"
                 "buffer_load_dwordx4 %0, %1, %2 offen offset:1024 sc1 lds" :: "v"(voff), "s"(srd), "s"(soff), "s"(lds) : "memory");
  if constexpr (N == 3)
    asm volatile("s_mov_b32 m0, %3\n\ts_nop 0\n\tbuffer_load_dwordx4 %0, %1, %2 offen sc1 lds\n\t"
                 "buffer_load_dwordx4 %0, %1, %2 offen offset:1024 sc1 lds\n\t"
                 "buffer_load_dwordx4 %0, %1, %2 offen offset:2048 sc1 lds" :: "v"(voff), "s"(srd), "s"(soff), "s"(lds) : "memory");
  if constexpr (N == 4)
    asm volatile("s_mov_b32 m0, %3\n\ts_nop 0\n\tbuffer_load_dwordx4 %0, %1, %2 offen sc1 lds\n\t"
                 "buffer_load_dwordx4 %0, %1, %2 offen offset:1024 sc1 lds\n\t"
                 "buffer_load_dwordx4 %0, %1, %2 offen offset:2048 sc1 lds\n\t"
                 "buffer_load_dwordx4 %0, %1, %2 offen offset:3072 sc1 lds" :: "v"(voff), "s"(srd), "s"(soff), "s"(lds) : "memory");
}
DI void dma_g(unsigned voff, const void* sbase, unsigned lds) {
  asm volatile("s_mov_b32 m0, %2\n\ts_nop 0\n\tglobal_load_lds_dwordx4 %0, %1" :: "v"(voff), "s"(sbase), "s"(lds) : "memory");
}

DI bf16x8 lds_rd128(unsigned a) { return *(LAS bf16x8*)(size_t)a; }
DI f32x4 lds_rdf4(unsigned a) { return *(LAS f32x4*)(size_t)a; }
DI bf16x4 lds_tr(unsigned a) { return __builtin_amdgcn_ds_read_tr16_b64_v4bf16((LAS bf16x4*)(size_t)a); }
DI bf16x8 cat8(bf16x4 lo, bf16x4 hi) { return __builtin_shufflevector(lo, hi, 0, 1, 2, 3, 4, 5, 6, 7); }
DI bf16x8 lds_trfrag(unsigned a, unsigned step) { return cat8(lds_tr(a), lds_tr(a + step)); }
DI unsigned pk2(float a, float b) {
  f32x2 v = {a, b};
  return __builtin_bit_cast(unsigned, __builtin_convertvector(v, bf16x2));
}
DI float bfr(float a) { return (float)(__bf16)a; }
DI float lo_f(unsigned u) { return __uint_as_float(u << 16); }
DI float hi_f(unsigned u) { return __uint_as_float(u & 0xffff0000u); }
DI bf16x8 bf8(u32x4 v) { return __builtin_bit_cast(bf16x8, v); }
DI f32x16 mfma32(bf16x8 a, bf16x8 b, f32x16 c) { return __builtin_amdgcn_mfma_f32_32x32x16_bf16(a, b, c, 0, 0, 0); }
DI f32x4 mfma16(bf16x8 a, bf16x8 b, f32x4 c) { return __builtin_amdgcn_mfma_f32_16x16x32_bf16(a, b, c, 0, 0, 0); }
DI f32x16 mfma8(u32x2 a, u32x2 b, f32x16 c) {
  return __builtin_amdgcn_mfma_f32_32x32x8bf16_1k(__builtin_bit_cast(s16x4, a), __builtin_bit_cast(s16x4, b), c, 0, 0, 0);
}
typedef __amdgpu_buffer_rsrc_t rsrc_t;
DI rsrc_t mkrsrc(const void* p, unsigned n) { return __builtin_amdgcn_make_buffer_rsrc((void*)p, 0, n, 0x00020000); }
DI void st128(rsrc_t r, unsigned vo, unsigned so, u32x4 v) {
  __builtin_amdgcn_raw_buffer_store_b128(v, r, vo, so, 16);
  asm volatile("s_nop 1" :: "v"(v) : "memory");
}
DI void st64(rsrc_t r, unsigned vo, unsigned so, u32x2 v) { __builtin_amdgcn_raw_buffer_store_b64(v, r, vo, so, 16); }
DI void st32(rsrc_t r, unsigned vo, unsigned so, unsigned v) { __builtin_amdgcn_raw_buffer_store_b32(v, r, vo, so, 16); }
DI u32x4 ld128(rsrc_t r, unsigned vo, unsigned so) { return __builtin_amdgcn_raw_buffer_load_b128(r, vo, so, 16); }
DI u32x2 ld64(rsrc_t r, unsigned vo, unsigned so) { return __builtin_amdgcn_raw_buffer_load_b64(r, vo, so, 16); }
DI unsigned ld32(rsrc_t r, unsigned vo, unsigned so) { return __builtin_amdgcn_raw_buffer_load_b32(r, vo, so, 16); }
DI u32x4 mkdesc(const void* p, unsigned n) {
  const unsigned long long a = (unsigned long long)p;
  return (u32x4){(unsigned)a, (unsigned)(a >> 32) & 0xffffu, n, 0x00020000u};
}
template <int CTRL> DI float dpp_add(float v) {
  return v + __uint_as_float(__builtin_amdgcn_update_dpp(0, __float_as_uint(v), CTRL, 0xf, 0xf, true));
}
DI float wave_sum(float v) {
  v = dpp_add<0x111>(v); v = dpp_add<0x112>(v); v = dpp_add<0x114>(v); v = dpp_add<0x118>(v);
  const float a = __uint_as_float(__builtin_amdgcn_readlane(__float_as_uint(v), 15));
  const float b = __uint_as_float(__builtin_amdgcn_readlane(__float_as_uint(v), 31));
  const float c = __uint_as_float(__builtin_amdgcn_readlane(__float_as_uint(v), 47));
  const float d = __uint_as_float(__builtin_amdgcn_readlane(__float_as_uint(v), 63));
  return (a + b) + (c + d);
}
DI int fresh_lane() {
  int l; asm volatile("v_mbcnt_lo_u32_b32 %0, -1, 0\n\tv_mbcnt_hi_u32_b32 %0, -1, %0" : "=v"(l)); return l;
}
DI int opq(int v) { asm volatile("" : "+s"(v)); return v; }
DI unsigned opq(unsigned v) { asm volatile("" : "+s"(v)); return v; }
DI unsigned vopq(unsigned v) { asm("" : "+v"(v)); return v; }
DI float vmul(float a, float b) { float r; asm("v_mul_f32 %0, %1, %2" : "=v"(r) : "v"(a), "v"(b)); return r; }
DI unsigned vadd(unsigned a, unsigned c) { unsigned r; asm volatile("v_add_u32 %0, %2, %1" : "=v"(r) : "v"(a), "s"(c)); return r; }
DI void lds_inc(unsigned a) {
  asm volatile("s_mov_b64 exec, 1\n\tds_add_u32 %0, %1\n\ts_mov_b64 exec, -1" :: "v"(a), "v"(1u) : "memory");
}
constexpr unsigned SPIN_CAP = 1u << 16;
DI void lds_wait(unsigned a, unsigned v) {
#pragma unroll 1
  for (unsigned sp = 0; sp < 64 * SPIN_CAP; ++sp) {
    if (__builtin_amdgcn_readfirstlane(*(volatile LAS unsigned*)(size_t)a) >= v) break;
    __builtin_amdgcn_s_sleep(1);
  }
  asm volatile("" ::: "memory");
}

constexpr unsigned XOFF = 98304, RTO = 73728, EX1 = 49152, EX2 = 51200;
constexpr unsigned xsl(int s) { return XOFF + (unsigned)(s & 3) * 16384u; }
constexpr unsigned wsl1(int s) { return (unsigned)(s & 3) * 24576u; }
constexpr unsigned wsl2(int u) {
  return u == 0 ? 57344u : u == 1 ? 73728u : u == 2 ? 0u : u == 3 ? 16384u : u == 4 ? 32768u :
         u == 5 ? 57344u : u == 6 ? 131072u : u == 7 ? 147456u : u == 8 ? 73728u : 98304u;
}
constexpr unsigned US = 8256, KO = 0, VO = 3 * US;
constexpr unsigned MISC = 61440, CNTA = MISC, CNTY = MISC + 4, CNTD = MISC + 8, RDYB = MISC + 16;
constexpr unsigned ONES = 55424, BO = MISC + 1024;
static_assert(6 * US <= EX2 && BO + 99 * 1024 == 163840 && (ONES & 255) == ((VO + 192) & 255) && ONES >= EX2 + 4096 && ONES + 5120 <= MISC, "lds");

constexpr int K_ST0 = 6, K_FL0 = 15, K_CS1 = 26, K_GR = 28, K_PL0 = 31, K_FL1 = 51, K_FH1 = 59;
constexpr int K_PL1 = 58, K_CS2 = 53, K_CH1 = 65, K_CH2 = 73, K_BH = 58, K_PH1 = 64, K_AH = 70;
struct Ops { short id[320]; short k[320]; short dma[320]; int n; };
constexpr int p2_at(int K) {
  return K == 29 ? 0 : K == 30 ? 1 : K == 31 ? 2 : K == 32 ? 3 : K == 35 ? 4 : K == 36 ? 5 : K == 39 ? 6 : K == 41 ? 7 :
         K == 43 ? 8 : K == 45 ? 9 : K == 47 ? 10 : K == 49 ? 11 : (K >= 50 && K <= 57) ? K - 38 : -1;
}
constexpr Ops make_ops(bool odd, bool y) {
  Ops o{}; int n = 0;
  auto add = [&](int id, int k, int d) { o.id[n] = (short)id; o.k[n] = (short)k; o.dma[n] = (short)d; ++n; };
  for (int i = 0; i < 5; ++i) add(100 + i, -3, 1);
  add(4, -3, 0);
  add(3, -3, 0); add(3, -3, 0); add(3, -3, 0);
  for (int i = 5; i < 10; ++i) add(100 + i, -3, 1);
  add(1, -3, 0); add(1, -3, 0);
  for (int i = 10; i < 15; ++i) add(100 + i, -3, 1);
  for (int K = 0; K < 64; ++K) {
    if (K >= 3 && K <= 30) {
      const int s = ((K - 3) >> 2) + 3, rel = (K - 3) & 3;
      if (rel == 0) { add(100 + 5 * s, K, 1); add(101 + 5 * s, K, 1); } else add(101 + 5 * s + rel, K, 1);
    }
    if (p2_at(K) >= 0) add(200 + p2_at(K), K, 1);
    if (K == K_ST0) for (int i = 0; i < 4; ++i) add(310, K, 0);
    if (K >= K_CS1 && K < K_CS1 + 5) add(325, K, 0);
    if (K == K_GR) add(320, K, 0);
    if (K == K_PL0) add(340, K, 0);
    if (K >= 40 && K < (odd ? 46 : 42)) add(330, K, 0);
    if (!odd && (K == 45 || K == 49)) add(331, K, 0);
    if (K == 44) add(332, K, 0);
    if (!y && K == K_PL1) add(350, K, 0);
    if (!y && K >= K_CS2 && K < K_CS2 + 5) add(350, K, 0);
    if (y && K >= K_BH) add(350, K, 0);
  }
  o.n = n; return o;
}
constexpr Ops OPS[4] = {make_ops(false, false), make_ops(true, false), make_ops(false, true), make_ops(true, true)};
constexpr bool stage_has(int id, int S) {
  return (id >= 100 && id < 200) ? (id - 100) / 5 <= S : (id >= 200 && id < 300) ? (id - 200) / 2 + 10 <= S : false;
}
constexpr int cmin(int a, int b) { return a < b ? a : b; }
constexpr int clampw(int w) { return w < 0 ? 0 : (w > 63 ? 63 : w); }
constexpr int wait_bar(const Ops& o, int K) {
  const int S = K == 63 ? 17 : (K >> 2) + 1;
  int issued = 0, last = -1;
  for (int i = 0; i < o.n; ++i) {
    if (o.k[i] < K) issued = i + 1;
    if (stage_has(o.id[i], S)) last = i;
    if (K == K_FL0 && o.id[i] == 310) last = last > i ? last : i;
    if (K == K_FL1 && o.id[i] == 330) last = last > i ? last : i;
    if (K == K_FH1 && o.id[i] == 331) last = last > i ? last : i;
  }
  return clampw(issued - 1 - last);
}
constexpr int wait_mid(const Ops& o, int K, int id) {
  int issued = 0, last = -1;
  for (int i = 0; i < o.n; ++i) { if (o.k[i] < K || (o.k[i] == K && o.dma[i])) issued = i + 1; if (o.id[i] == id) last = i; }
  return clampw(issued - 1 - last);
}

__global__ __launch_bounds__(512, 2) void fused(const char* __restrict__ x, const char* __restrict__ wqkv,
                                                const char* __restrict__ wo, const char* __restrict__ wg,
                                                const char* __restrict__ wu, const char* __restrict__ wd,
                                                char* __restrict__ ws, char* __restrict__ out) {
  const int b = blockIdx.x;
  const int mt = 8 * (b & 1) + (b >> 5), g = 4 * ((b >> 1) & 3) + ((b >> 3) & 3), odd = g & 1, p = g >> 1;
  const int w = __builtin_amdgcn_readfirstlane(threadIdx.x >> 6);
  const int rgp = w & 3, nlo = w >> 2;
  int lane = threadIdx.x & 63, h = lane >> 5, n = lane & 31;
  unsigned l16 = lane * 16;
#define REFRESH() do { lane = fresh_lane(); h = lane >> 5; n = lane & 31; l16 = lane * 16; } while (0)
  const int own = (mt & 7) + 2 * (mt >> 3);
  const int r0 = (own + ((0x8530 >> (4 * (g & 3))) & 15) + 2 * (g >> 2)) % 10;
  STAMP_DECL; STAMP(0);

  const unsigned wb1 = w * 1024, wb2 = w * 2048;
  const u32x4 sdx = mkdesc(x + (size_t)(128 * mt + 16 * w) * 1280, 0x40000000u);
  const u32x4 sdq = mkdesc(wqkv + (size_t)(8 * w) * 3840, 0x40000000u);
  const u32x4 sd1 = mkdesc(odd ? wqkv + (size_t)(8 * w) * 3840 : wg + (size_t)(8 * w) * 3072, 0x40000000u);
  const u32x4 sd2 = mkdesc(odd ? wqkv + (size_t)(8 * w) * 3840 : wu + (size_t)(8 * w) * 3072, 0x40000000u);
  const unsigned step12 = odd ? 245760u : 196608u;
  unsigned xv0, xv1, wv[3];
  {
    const int r = lane >> 3, u = lane & 7, sw = ((r >> 1) & 1) << 2;
    xv0 = r * 1280 + ((u ^ (r >> 1)) << 4);
    xv1 = (r + 8) * 1280 + ((u ^ (4 + (r >> 1))) << 4) - 1024;
    const unsigned vq = r * 3840 + ((u ^ sw) << 4);
    wv[0] = vq + (4 * p + odd) * 128;
    wv[1] = odd ? vq + (p == 7 ? 29 : 4 * p + 2) * 128 : r * 3072 + ((u ^ sw) << 4) + 3 * p * 128;
    wv[2] = odd ? vq + (p == 7 ? 29 : 4 * p + 3) * 128 : r * 3072 + ((u ^ sw ^ 2) << 4) + 3 * p * 128;
  }
  auto kst = [&](int pos) { int k = r0 + pos; return (unsigned)(k >= 10 ? k - 10 : k); };
  auto issue1 = [&](auto SS, auto RR) {
    constexpr int s = SS, rel = RR;
    if constexpr (rel == 0) dma_x<xsl(s)>(xv0, xv1, sdx, kst(s), wb2);
    else dma_k<wsl1(s) + (rel - 1) * 8192>(wv[rel - 1], rel == 1 ? sdq : rel == 2 ? sd1 : sd2, kst(s), rel == 1 ? 245760u : step12, wb1);
  };
  PSTAMP(7);
  static_for<4>([&](auto I) { issue1(IC(0), I); });
  SB();
  PSTAMP(8);
  barrier();
  STAMP(1);
  unsigned pfd;
  {
    unsigned long long pc; unsigned len, l2;
    asm volatile("s_getpc_b64 %0\n1:\ts_mov_b32 %1, fused_lx-1b\n\ts_mov_b32 %2, fused_ly-1b\n\ts_max_u32 %1, %1, %2"
                 : "=s"(pc), "=s"(len), "=s"(l2) :: "scc");
    const u32x4 sdc = {(unsigned)pc, (unsigned)(pc >> 32) & 0xffffu, len, 0x00020000u};
    asm volatile("buffer_load_dword %0, %1, %2, %3 offen" : "=v"(pfd) : "v"(lane < 2 ? lane * 128 : OOB), "s"(sdc), "s"(((b >> 3) * 8 + w) * 256) : "memory");
  }
  u32x4 sx4; unsigned sx1, gen;
  const int srow = 128 * mt + 8 * g + w;
  {
    const char* rp = x + (size_t)srow * 1280;
    asm volatile("global_load_dwordx4 %0, %1, %2" : "=v"(sx4) : "v"(lane * 16), "s"(rp) : "memory");
    asm volatile("global_load_dword %0, %1, %2 offset:1024" : "=v"(sx1) : "v"(lane * 4), "s"(rp) : "memory");
    asm volatile("global_load_dword %0, %1, %2 sc1" : "=v"(gen) : "v"(0), "s"(ws + GEN_OFF) : "memory");
  }
  static_for<4>([&](auto I) { issue1(IC(1), I); });
  const int jok = b < 170, jb = jok ? b : b - 170;
  const int jG = (jb * 241) >> 12, jks = (jb - 17 * jG) * 4 + (w >> 1);
  {
    const char* src = (jks < 48 ? wd + jks * (32 * 1280) : wo + (jks - 48) * (32 * 1280)) + (w & 1) * (16 * 1280) + jG * 128;
    const unsigned vo = (lane >> 3) * 1280 + (lane & 7) * 16;
    dma_g(vo, src, RTO + wb2);
    dma_g(vo, src + 8 * 1280, RTO + wb2 + 1024);
  }
  static_for<4>([&](auto I) { issue1(IC(2), I); });
  SB();
  PSTAMP(9);

  unsigned wa1[3], wa2[2], xa[4];
  {
    const int cb = (lane >> 4) & 1, q = (lane >> 2) & 3, pl = (lane >> 1) & 1, ph = lane & 1;
    const unsigned rowoff = (8 * h + q) * 128 + pl * 8;
    const int sw = (q >> 1) << 2;
#pragma unroll
    for (int i = 0; i < 3; ++i) {
      const int j = 3 * nlo + i;
      const int piece = odd ? (j >> 1) : 0, sel = odd ? (j & 1) : nlo, t = 2 * nlo + i - 1;
      const unsigned aq = piece * 8192 + (((4 * sel + 2 * cb + ph) ^ sw) << 4);
      const unsigned ag = (1 + cb) * 8192 + (((2 * t + ph) ^ sw ^ (cb << 1)) << 4);
      wa1[i] = rowoff + ((odd || i == 0) ? aq : ag);
    }
#pragma unroll
    for (int i = 0; i < 2; ++i) wa2[i] = rowoff + cb * 8192 + (((2 * (2 * nlo + i) + ph) ^ sw ^ (cb << 1)) << 4);
    const int row = 32 * rgp + n, sx = (row >> 1) & 7;
#pragma unroll
    for (int j = 0; j < 4; ++j) xa[j] = XOFF + row * 128 + (((2 * j + h) ^ sx) << 4);
  }
  auto mksel = [&]() {
    const int i = lane & 15, kg = lane >> 4;
    const unsigned o1 = ((i & 3) | (((i >> 2) ^ kg) & 1)) == 0 ? 0x3f803f80u : 0u;
    return bf8((u32x4){o1, o1, o1, o1});
  };
  bf16x8 selv = mksel();
  const rsrc_t rws = mkrsrc(ws, WS_END);
  const u32x4 sdw = mkdesc(ws, WS_END);
  SB();
  PSTAMP(10);
  wait_vm<16>();
  PSTAMP(14);
  barrier();
  STAMP(2);

  f32x16 acc1[3], acc2[2];
  f32x4 cs = (f32x4)(0.f);
  bf16x8 xr[56];
  bf16x8 wf[2][3], csf[2];
  unsigned wcs = rgp == 0 ? vopq(wa1[0]) : rgp == 1 ? vopq(wa1[1]) : vopq(wa1[2]);
  unsigned wcur[3];
  bf16x8 rt[2];
  u32x2 grn, cl[5];
  unsigned tag, nm;
  float smean;
  bf16x8 acs[3];
  float rstd, sdv, gsc;
  u32x4 pol, hq, sdg, sdu;
  unsigned pol1, st1 = 0, rdyh = 0, vs, vh, gv2, uv2;
  u32x4 q1[2]; u32x2 q2[2];
  u32x4 bq[12][5], aq[12][4];

  auto rd = [&](auto KK) {
    constexpr int K = KK;
    if constexpr (K < 40) {
      constexpr int s = K >> 2, j = K & 3;
      xr[K] = lds_rd128(xa[j] + (xsl(s) - XOFF));
      if constexpr (j == 0 && (s & 3) == 3) {
#pragma unroll
        for (int i = 0; i < 3; ++i) wcur[i] = vadd(wa1[i], wsl1(s));
      }
#pragma unroll
      for (int i = 0; i < 3; ++i) wf[K & 1][i] = lds_trfrag(((s & 3) == 3 ? wcur[i] : wa1[i] + wsl1(s)) + j * 2048, 512);
      if constexpr (K < 4) csf[K & 1] = lds_trfrag(wcs + wsl1(0) + j * 2048, 512);
    } else if constexpr (K < 80) {
      constexpr int u = (K - 40) >> 2, j = K & 3;
      if constexpr (u < 4) xr[K] = lds_rd128(xa[j] + (xsl(6 + u) - XOFF));
      constexpr bool far = wsl2(u) > 57344;
      if constexpr (j == 0 && far) {
#pragma unroll
        for (int i = 0; i < 2; ++i) wcur[i] = vadd(wa2[i], wsl2(u));
      }
#pragma unroll
      for (int i = 0; i < 2; ++i) wf[K & 1][i] = lds_trfrag((far ? wcur[i] : wa2[i] + wsl2(u)) + j * 2048, 512);
      if constexpr (K < 44) csf[K & 1] = lds_trfrag(wcs + wsl2(0) + j * 2048, 512);
    }
  };
  auto cs_ok = [&]() { return (((cl[0][1] ^ tag) | (cl[1][1] ^ tag)) | ((cl[2][1] ^ tag) | (cl[3][1] ^ tag)) | (cl[4][1] ^ tag)) == 0; };
  auto cs_fin = [&](unsigned src, unsigned dst) {
    if (!__all(cs_ok())) {
#pragma unroll 1
      for (unsigned sp = 0; sp < SPIN_CAP; ++sp) {
        __builtin_amdgcn_s_sleep(4);
#pragma unroll
        for (int t = 0; t < 5; ++t) cl[t] = ld64(rws, h * 1280 + n * 8 + t * 256, src);
        if (__all(cs_ok())) break;
      }
    }
    const float c = ((__uint_as_float(cl[0][0]) + __uint_as_float(cl[1][0])) + (__uint_as_float(cl[2][0]) + __uint_as_float(cl[3][0]))) + __uint_as_float(cl[4][0]);
    const unsigned u0 = __float_as_uint(c);
    const float r1 = c - __uint_as_float(u0 & 0xffff0000u);
    const unsigned u1 = __float_as_uint(r1);
    const float r2 = r1 - __uint_as_float(u1 & 0xffff0000u);
    *(LAS u32x4*)(size_t)(dst + l16) = (u32x4){__builtin_amdgcn_perm(u1, u0, 0x07060302u), __float_as_uint(r2) >> 16, 0u, 0u};
  };
  auto qkv_st = [&](const f32x16& a16, int cu0, int a) {
    const float r = rstd * (cu0 < 80 ? QSCALE : 1.f);
    u32x4 val;
#pragma unroll
    for (int i = 0; i < 4; ++i) val[i] = pk2(vmul(a16[8 * a + 2 * i], r), vmul(a16[8 * a + 2 * i + 1], r));
    st128(rws, vs, QKV_OFF + (cu0 + 2 * a) * 32768 + (128 * mt + 32 * rgp) * 16, val);
  };
  auto gu2 = [&](const f32x16& a16, int i) {
    float t0, t1; unsigned r;
    asm("v_mul_f32 %1, %3, %7\n\tv_mul_f32 %2, %4, %7\n\tv_exp_f32 %1, %1\n\tv_exp_f32 %2, %2\n\t"
        "v_fma_f32 %1, %1, %8, %8\n\tv_fma_f32 %2, %2, %8, %8\n\tv_rcp_f32 %1, %1\n\tv_rcp_f32 %2, %2\n\t"
        "v_mul_f32 %1, %5, %1\n\tv_mul_f32 %2, %6, %2\n\tv_cvt_pk_bf16_f32 %0, %1, %2"
        : "=v"(r), "=&v"(t0), "=&v"(t1)
        : "v"(a16[2 * i]), "v"(a16[2 * i + 1]), "v"(a16[8 + 2 * i]), "v"(a16[9 + 2 * i]), "v"(gsc), "v"(sdv));
    hq[i] = r;
  };
  auto gu_st = [&](int unit, int t) {
    st128(rws, vh, A2H_OFF + (((2 * mt + (rgp >> 1)) * 48 + 2 * unit + (t >> 1)) * 4 + 2 * (rgp & 1)) * 1024 + (t & 1) * 512, hq);
  };
  auto kv_piece = [&](int c) {
    const int bb = opq(b), pair = 2 * (bb & 7) + ((bb >> 4) & 1), e = (bb >> 3) & 1, QB = bb >> 5;
    int t0 = 256 * QB - 256 + 128 * rgp; t0 = t0 < 0 ? 0 : t0;
    if (c < 6) dma_lin<2>(l16, sdw, QKV_OFF + (80 + 80 * (c / 3) + 5 * pair + 2 * e + c % 3) * 32768 + t0 * 16, c * US + rgp * 2048);
    else {
      const unsigned so = QKV_OFF + 5 * pair * 32768 + (256 * QB + 64 * rgp + 32 * (c - 6)) * 16;
      q1[c - 6] = ld128(rws, vs, so + 3 * e * 32768);
      q2[c - 6] = ld64(rws, h ? OOB : n * 16, so + 2 * 32768 + 8 * e);
    }
  };
  auto pol1_ld = [&]() {
    const int bb = opq(b);
    const bool on = (lane < 24) & (lane >= (bb >= 32 ? 0 : 12));
    const unsigned v = ld32(rws, on ? lane * 4 : OOB, F1_OFF + ((bb & 7) * 8 + (bb >> 5)) * 128);
    return on ? v : tag;
  };
  unsigned av[4], l16b, kt1, kt2, abase, bbase, soa, sob;
  auto ksof = [&](int s) { return ((s < 4 ? opq(kt1) >> (4 * s) : opq(kt2) >> (4 * (s - 4))) & 15u); };
  auto ldB = [&](int s, int c) {
    if (c == 0) sob = bbase + ksof(s) * 5120;
    bq[s][c] = c < 4 ? ld128(rws, l16 + c * 1024, sob) : ld128(rws, l16b, sob);
  };
  auto ldA = [&](int s, int r) {
    if (r == 0) soa = abase + ksof(s) * 4096;
    aq[s][r] = ld128(rws, av[r], soa);
  };
  auto polh_ld = [&]() {
    const int bb = opq(b);
    return ld32(rws, (lane & 1) * 4, FH_OFF + (2 * (bb & 7) + (bb >> 7)) * 128 + rgp * 8);
  };

  auto step = [&](auto KK, auto OD, auto RL) {
    constexpr int K = KK, j = K & 3, ODD = OD, ROLE = RL;
    if constexpr (K == 40) STAMP(3);
    if constexpr (j == 3 && K <= 63) {
      constexpr int nx = ODD < 2 ? wait_bar(OPS[ODD & 1], K) : cmin(wait_bar(OPS[0], K), wait_bar(OPS[1], K));
      constexpr int ny = ODD < 2 ? wait_bar(OPS[2 + (ODD & 1)], K) : cmin(wait_bar(OPS[2], K), wait_bar(OPS[3], K));
      static_assert(ROLE < 2 || nx == ny, "");
      wait_vm_l0<(ROLE == 1 ? ny : nx)>();
      barrier();
      if constexpr (K == K_FL0) { if (w == 0) st32(rws, fresh_lane() == 0 ? 0u : OOB, F0_OFF + b * 4, tag); }
      if constexpr (K == K_FL1) {
        if (w == 0) {
          const int l = fresh_lane(), ui = l >> 2, hi = (l >> 1) & 1, qi = l & 1;
          const int u = 4 * p + odd + ui;
          const int G = ((u * 4 * 205) >> 10) + hi;
          const int fu = (5 * G) >> 2;
          const int qb = (mt >> 1) + qi;
          const bool on = (ui < (odd ? 3 : 1)) & (u < 30) & (fu <= u) & (qb < 8);
          const unsigned a = ((G & 7) * 8 + qb) * 128 + ((mt - 2 * qb + 2) * 6 + (G >> 3) * 2 + (u - fu)) * 4;
          st32(rws, on ? a : OOB, F1_OFF, tag);
        }
      }
      if constexpr (K == K_FH1 && ROLE == 0) { if (w == 0 && !odd) st32(rws, fresh_lane() == 0 ? 0u : OOB, FH_OFF + mt * 128 + p * 4, tag); }
    }
    if constexpr (K == 71) { wait_l0(); barrier(); }
    rd(IC(K + 1));
    if constexpr (K >= 3 && K <= 30) issue1(IC(((K - 3) >> 2) + 3), IC((K - 3) & 3));
    if constexpr (p2_at(K) >= 0) {
      constexpr int u = p2_at(K) >> 1, jj = p2_at(K) & 1;
      dma_k<wsl2(u) + jj * 8192>(jj ? uv2 : gv2, jj ? sdu : sdg, kst((u + 6) % 10), 196608u, wb1);
    }
    if constexpr (K < 40) {
#pragma unroll
      for (int i = 0; i < 3; ++i) acc1[i] = mfma32(wf[K & 1][i], xr[K], K == 0 ? (f32x16)(0.f) : acc1[i]);
      __builtin_amdgcn_sched_group_barrier(0x8, 1, 0); __builtin_amdgcn_sched_group_barrier(0x100, 3, 0);
      __builtin_amdgcn_sched_group_barrier(0x8, 1, 0); __builtin_amdgcn_sched_group_barrier(0x100, 2, 0);
      __builtin_amdgcn_sched_group_barrier(0x8, 1, 0); __builtin_amdgcn_sched_group_barrier(0x100, 2, 0);
    } else {
      constexpr int u = (K - 40) >> 2;
#pragma unroll
      for (int i = 0; i < 2; ++i) acc2[i] = mfma32(wf[K & 1][i], xr[u < 4 ? K : K - 56], K == 40 ? (f32x16)(0.f) : acc2[i]);
      __builtin_amdgcn_sched_group_barrier(0x8, 1, 0); __builtin_amdgcn_sched_group_barrier(0x100, 3, 0);
      __builtin_amdgcn_sched_group_barrier(0x8, 1, 0); __builtin_amdgcn_sched_group_barrier(0x100, 5, 0);
    }
    SB();
    if constexpr (K < 40) asm volatile("" :: "v"(acc1[0]), "v"(acc1[1]), "v"(acc1[2]));
    else asm volatile("" :: "v"(acc2[0]), "v"(acc2[1]));
    if constexpr (K < 4 || (K >= 40 && K < 44)) cs = mfma16(selv, csf[K & 1], cs);
    if constexpr (K == 0) {
      asm volatile("s_waitcnt vmcnt(%3)" : "+v"(sx4), "+v"(sx1), "+v"(gen) : "i"(wait_mid(OPS[0], K, 3)) : "memory");
      tag = __builtin_amdgcn_readfirstlane(gen) * 0x9E3779B1u + 0x7F4A7C15u;
      float s = lo_f(sx1) + hi_f(sx1);
#pragma unroll
      for (int i = 0; i < 4; ++i) s += lo_f(sx4[i]) + hi_f(sx4[i]);
      smean = wave_sum(s) * (1.f / 640.f);
    }
    if constexpr (K == 2) {
      wait_vm<wait_mid(OPS[0], K, 1)>();
      const unsigned a = RTO + wb2 + ((lane >> 2) & 3) * 128 + (lane >> 4) * 32 + (lane & 3) * 8;
      rt[0] = lds_trfrag(a, 512); rt[1] = lds_trfrag(a + 1024, 512);
    }
    if constexpr (K == 3) asm volatile("" : "+v"(rt[0]), "+v"(rt[1]) :: "memory");
    if constexpr (K == 4) {
      float q = 0.f;
#pragma unroll
      for (int i = 0; i < 5; ++i) {
        const unsigned u = i < 4 ? sx4[i] : sx1;
        const float d0 = lo_f(u) - smean, d1 = hi_f(u) - smean;
        q = __builtin_fmaf(d0, d0, q); q = __builtin_fmaf(d1, d1, q);
      }
      const float var = wave_sum(q) * (1.f / 640.f);
      grn[0] = pk2(smean, __builtin_amdgcn_sqrtf(bfr(bfr(var) + 1e-5f)));
      grn[1] = tag;
    }
    if constexpr (K == K_ST0 || K == 44) {
      REFRESH();
      const bool on = lane < ((rgp < (K == 44 ? 2 : 3) && (mt < 8 || mt >= 14)) ? 32 : 0);
      st64(rws, on ? n * 8 : OOB, CS_OFF + ((g * 10 + (K == 44 ? 6 + 2 * nlo : 3 * nlo) + rgp) * 10 + own) * 256,
           (u32x2){__float_as_uint(cs[0]), tag});
    }
    if constexpr (K == K_ST0) {
      st64(rws, lane == 0 ? 0u : OOB, GR_OFF + srow * 8, grn);
      const int f = 4 * jG + (lane >> 4), ct = (f * 205) >> 10, c = f - 5 * ct;
      const unsigned vo = jok ? (ct * (jks < 48 ? 240 : 100) + c) * 1024 + (lane & 15) * 16 : OOB;
      const unsigned so = (jks < 48 ? B2H_OFF + jks * 5120 : B2O_OFF + (jks - 48) * 5120) + (w & 1) * 512;
      st128(rws, vo, so, __builtin_bit_cast(u32x4, rt[0]));
      st128(rws, vo + 256, so, __builtin_bit_cast(u32x4, rt[1]));
    }
    if constexpr (K == 24) {
      REFRESH();
      const int r = lane >> 3, u = lane & 7, sw = ((r >> 1) & 1) << 2;
      gv2 = r * 3072 + ((u ^ sw) << 4) + (3 * p + 1 + odd) * 128;
      uv2 = r * 3072 + ((u ^ sw ^ 2) << 4) + (3 * p + 1 + odd) * 128;
    }
    if constexpr (K == 28) sdg = mkdesc(wg + (size_t)(8 * opq(w)) * 3072, 0x40000000u);
    if constexpr (K == 29) sdu = mkdesc(wu + (size_t)(8 * opq(w)) * 3072, 0x40000000u);
    if constexpr (K >= K_CS1 && K < K_CS1 + 5) {
      if constexpr (K == K_CS1) { REFRESH(); vs = w < 6 ? h * 1280 + n * 8 : OOB; }
      asm volatile("buffer_load_dwordx2 %0, %1, %2, %3 offen offset:%4 sc1" : "=v"(cl[K - K_CS1])
                   : "v"(vs), "s"(sdw), "s"(CS_OFF + (g * 10 + w) * 2560), "i"((K - K_CS1) * 256) : "memory");
    }
    if constexpr (K == K_GR)
      asm volatile("buffer_load_dwordx2 %0, %1, %2, %3 offen sc1" : "=v"(grn) : "v"(n * 8), "s"(sdw), "s"(GR_OFF + (128 * mt + 32 * rgp) * 8) : "memory");
    if constexpr (K == K_PL0)
      asm volatile("buffer_load_dwordx4 %0, %1, %2, %3 offen sc1" : "=v"(pol) : "v"(w == 7 ? l16 : OOB), "s"(sdw), "s"(F0_OFF) : "memory");
    if constexpr (K == 34) {
      asm volatile("s_waitcnt vmcnt(%6)" : "+v"(grn), "+v"(cl[0]), "+v"(cl[1]), "+v"(cl[2]), "+v"(cl[3]), "+v"(cl[4])
                   : "i"(wait_mid(OPS[0], K, 325)) : "memory");
      if (w < 6) cs_fin(CS_OFF + (g * 10 + w) * 2560, EX1 + wb1);
      if (!__all(grn[1] == tag)) {
#pragma unroll 1
        for (unsigned sp = 0; sp < SPIN_CAP; ++sp) {
          __builtin_amdgcn_s_sleep(4);
          grn = ld64(rws, n * 8, GR_OFF + (128 * mt + 32 * rgp) * 8);
          if (__all(grn[1] == tag)) break;
        }
      }
      nm = ((grn[0] & 0xffffu) ^ 0x8000u) * 0x10001u;
      sdv = hi_f(grn[0]);
      rstd = __builtin_amdgcn_rcpf(sdv);
      gsc = -LOG2E * rstd;
    }
    if constexpr (K == 35) {
#pragma unroll
      for (int i = 0; i < 3; ++i) acs[i] = lds_rd128(EX1 + (3 * nlo + i) * 1024 + l16);
    }
    if constexpr (K >= 36 && K < 39) acc1[K - 36] = mfma32(acs[K - 36], bf8((u32x4){nm, nm, nm, nm}), acc1[K - 36]);
    if constexpr (K == 38) {
      wcs = (rgp & 1) ? vopq(wa2[1]) : vopq(wa2[0]);
      asm volatile("s_waitcnt vmcnt(%1)" : "+v"(pol) : "i"(wait_mid(OPS[0], K, 340)) : "memory");
      if (w == 7) {
        auto ok = [&]() { return ((pol[0] ^ tag) | (pol[1] ^ tag) | (pol[2] ^ tag) | (pol[3] ^ tag)) == 0; };
        if (!__all(ok())) {
#pragma unroll 1
          for (unsigned sp = 0; sp < SPIN_CAP; ++sp) {
            __builtin_amdgcn_s_sleep(4);
            pol = ld128(rws, l16, F0_OFF);
            if (__all(ok())) break;
          }
        }
        if (b == 0) st32(rws, lane == 0 ? 0u : OOB, GEN_OFF, tag);
      }
    }
    if constexpr (K == 39) {
      REFRESH();
      vs = h * 32768 + n * 16;
      vh = (n >> 4) * 1024 + h * 256 + (n & 15) * 16;
      selv = mksel();
      cs = (f32x4)(0.f);
    }
    if constexpr (K == 40 || K == 41) qkv_st(acc1[0], odd ? 32 * p + 8 + 12 * nlo : 32 * p + 4 * nlo, K & 1);
    if constexpr (K >= 42 && K < 50) {
      constexpr int z = K - 42;
      if (odd) {
        if constexpr (K < 46) qkv_st(acc1[(K - 40) >> 1], 32 * p + 8 + 12 * nlo + 4 * ((K - 40) >> 1), K & 1);
      } else {
        gu2(acc1[1 + (z >> 2)], z & 3);
        if constexpr ((z & 3) == 3) gu_st(3 * p, 2 * nlo + (z >> 2));
      }
    }
    if constexpr (K == K_CS2 - 1) {
      REFRESH();
      if constexpr (ROLE == 1) {
#pragma unroll
        for (int r = 0; r < 4; ++r) av[r] = l16 + ((r + rgp) & 3) * 1024;
        l16b = l16 + 4096;
        const int bb = opq(b), ct = (bb >> 3) & 7;
        kt1 = 0x76107610u >> (4 * (ct & 3));
        kt2 = (unsigned)(0xBA985432BA985432ull >> (4 * ((ct + 2 * (bb >> 6)) & 7)));
        abase = A2H_OFF + ((4 * (bb & 7) + (bb >> 6)) * 48 + 12 * rgp) * 4096;
        bbase = B2H_OFF + (ct * 48 + 12 * rgp) * 5120;
      }
    }
    if constexpr (K >= K_CS2) {
      if constexpr (ROLE == 0) {
        if constexpr (K < K_CS2 + 5) cl[K - K_CS2] = ld64(rws, h * 1280 + n * 8 + (K - K_CS2) * 256, CS_OFF + (g * 10 + 6 + w) * 2560);
        if constexpr (K == K_PL1) pol1 = pol1_ld();
        if constexpr (K == K_CH1) {
          st1 = __all(pol1 == tag) ? 1 : 0;
          cs_fin(CS_OFF + (g * 10 + 6 + w) * 2560, EX2 + wb1);
        }
        if constexpr (K == K_CH1 + 1) { if (st1 == 0) pol1 = pol1_ld(); }
        if constexpr (K == K_CH2) { if (st1 == 0 && __all(pol1 == tag)) st1 = 2; }
        if constexpr (K >= K_CH1 && K < K_CH1 + 8) { if (st1 == 1) kv_piece(K - K_CH1); }
        if constexpr (K >= K_CH2 && K < K_CH2 + 7) { if (st1 == 2) { kv_piece(K - K_CH2); if constexpr (K == K_CH2 + 6) kv_piece(7); } }
      } else {
        if constexpr (K >= K_BH && K < K_BH + 20) ldB((K - K_BH) / 5, (K - K_BH) % 5);
        if constexpr (K == K_PH1) pol1 = polh_ld();
        if constexpr (K == K_AH) rdyh = __all(pol1 == tag) ? 1 : 0;
        if constexpr (K >= K_AH && K < K_AH + 8) { if (rdyh) { ldA((K - K_AH) >> 1, 2 * (K & 1)); ldA((K - K_AH) >> 1, 2 * (K & 1) + 1); } }
      }
    }
    if constexpr (K == 71) {
#pragma unroll
      for (int i = 0; i < 2; ++i) acs[i] = lds_rd128(EX2 + (2 * nlo + i) * 1024 + l16);
    }
    if constexpr (K == 72 || K == 73) acc2[K - 72] = mfma32(acs[K - 72], bf8((u32x4){nm, nm, nm, nm}), acc2[K - 72]);
    if constexpr (K == 74) {
      const unsigned o1 = w < 5 ? 0x3f803f80u : 0u;
      if (w < 6) *(LAS u32x4*)(size_t)((w < 5 ? ONES + wb1 : MISC) + l16) = (u32x4){o1, o1, o1, o1};
    }
    SB();
  };
  rd(IC(0));
  static_rng<0, 52>([&](auto KK) { step(KK, IC(2), IC(2)); });

  auto epi2 = [&]() {
    STAMP(4);
    asm volatile("s_nop 7\n\ts_nop 7" : "+v"(acc2[0]), "+v"(acc2[1]));
#pragma unroll
    for (int i = 0; i < 2; ++i) {
#pragma unroll
      for (int z = 0; z < 4; ++z) gu2(acc2[i], z);
      gu_st(3 * p + 1 + odd, 2 * nlo + i);
    }
    SB();
  };
#define TAIL_COORDS \
  REFRESH(); \
  const int bb = opq(b), ct = (bb >> 3) & 7, rg = 4 * (bb & 7) + (bb >> 6); \
  const unsigned f2w = F2_OFF + (2 * mt + (rgp >> 1)) * 256 + (g * 4 + (rgp & 1) * 2 + nlo) * 4

  if (nlo == 0) {
    static_rng<52, 80>([&](auto KK) { step(KK, IC(2), IC(0)); });
    epi2();
    if (st1 == 0) {
#pragma unroll 1
      for (unsigned sp = 0; sp < SPIN_CAP; ++sp) {
        pol1 = pol1_ld();
        if (__all(pol1 == tag)) break;
        __builtin_amdgcn_s_sleep(4);
      }
      static_for<8>([&](auto I) { kv_piece(I); });
      wait_vm<0>();
    } else wait_vm<2>();
    wait_l0();
    asm volatile("" :: "v"(pfd));
    barrier();
    STAMP(5);
    __builtin_amdgcn_s_setprio(3);
    TAIL_COORDS;
    const int e = (bb >> 3) & 1, QB = bb >> 5, pair = 2 * (bb & 7) + ((bb >> 4) & 1);
    const int jmin = QB ? 0 : 8 - 2 * rgp;
    const unsigned ka1 = KO + (e + h) * US + (64 * rgp + n) * 16;
    const unsigned ka2 = KO + (e ? 8 : 2 * US) + (64 * rgp + n) * 16;
    unsigned va;
    {
      const int g1 = (lane >> 4) & 1, q = (lane >> 2) & 3, pp = lane & 3;
      const unsigned um = (e + (pp & 1)) * US + (pp >> 1) * 8, ut = e ? 8 : 2 * US;
      unsigned uo = VO + (g1 ? ut : um) + rgp * 1024;
      asm volatile("" : "+v"(uo));
      va = ((g1 * pp) ? ONES : uo) + (4 * h + q) * 16;
    }
    auto rdK1 = [&](int j) { return lds_rd128(ka1 + j * 512); };
    auto rdK2 = [&](int j) { return *(LAS u32x2*)(size_t)(ka2 + j * 512); };
    auto rdV = [&](int j, int sh) { return lds_trfrag(va + j * 512 + sh * 256, 128); };
    bf16x8 k1[2], vf[2][2]; u32x2 k2[2];
    k1[1] = rdK1(9); k2[1] = rdK2(9); k1[0] = rdK1(8); k2[0] = rdK2(8);
    vf[1][0] = rdV(9, 0); vf[1][1] = rdV(9, 1);
    f32x16 cc[2], o[2], sc[2][2];
#pragma unroll
    for (int a = 1; a >= 0; --a) {
      const u32x4 ku = __builtin_bit_cast(u32x4, k1[a]);
      float t = 0.f;
#pragma unroll
      for (int i = 0; i < 6; ++i)
        t = __builtin_amdgcn_fdot2_f32_bf16(__builtin_bit_cast(bf16x2, i < 4 ? ku[i] : k2[a][i - 4]),
                                            __builtin_bit_cast(bf16x2, i < 4 ? q1[a][i] : q2[a][i - 4]), t, false);
      auto r = __builtin_amdgcn_permlane32_swap(__float_as_uint(t), __float_as_uint(t), false, false);
      cc[a] = (f32x16)(-(__uint_as_float(r[0]) + __uint_as_float(r[1])));
      o[a] = (f32x16)(0.f);
    }
    sc[1][1] = mfma8(k2[1], q2[1], mfma32(k1[1], bf8(q1[1]), cc[1]));
    unsigned mk[8];
#pragma unroll
    for (int r = 0; r < 8; ++r) {
      const int c = n - 4 * h - ((2 * r & 3) + 8 * (r >> 1));
      mk[r] = (c >= 0 ? 0xffffu : 0u) | (c >= 1 ? 0xffff0000u : 0u);
    }
    SB();
    auto expk = [&](const f32x16& s, int sh, int mode) {
      u32x4 pu;
#pragma unroll
      for (int r = 0; r < 4; ++r) {
        pu[r] = pk2(__builtin_amdgcn_exp2f(s[8 * sh + 2 * r]), __builtin_amdgcn_exp2f(s[8 * sh + 2 * r + 1]));
        if (mode == 1) pu[r] &= mk[4 * sh + r];
        if (mode == 2) pu[r] &= ~mk[4 * sh + r];
      }
      return bf8(pu);
    };
    static_for<10>([&](auto TT) {
      constexpr int J = 9 - TT, cu = J & 1, nx = cu ^ 1;
      constexpr bool hasA = J <= 8, hasB = J >= 1, nA = J >= 1, nB = J >= 2;
      constexpr int mA = J == 8 ? 1 : J == 0 ? 2 : 0, mB = J == 9 ? 1 : J == 1 ? 2 : 0;
      if (J >= jmin) {
        bf16x8 pp;
        if constexpr (nB) sc[nx][1] = mfma32(k1[nx], bf8(q1[1]), cc[1]);
        if constexpr (hasA) pp = expk(sc[cu][0], 0, mA);
        if constexpr (nA) sc[nx][0] = mfma32(k1[nx], bf8(q1[0]), cc[0]);
        if constexpr (J >= 2) k1[cu] = rdK1(J - 2);
        if constexpr (J >= 1) vf[nx][0] = rdV(J - 1, 0);
        SB();
        if constexpr (hasA) { o[0] = mfma32(vf[cu][0], pp, o[0]); pp = expk(sc[cu][0], 1, mA); }
        if constexpr (nB) sc[nx][1] = mfma8(k2[nx], q2[1], sc[nx][1]);
        if constexpr (J >= 1) vf[nx][1] = rdV(J - 1, 1);
        SB();
        if constexpr (hasA) o[0] = mfma32(vf[cu][1], pp, o[0]);
        if constexpr (hasB) pp = expk(sc[cu][1], 0, mB);
        if constexpr (nA) sc[nx][0] = mfma8(k2[nx], q2[0], sc[nx][0]);
        if constexpr (J >= 2) k2[cu] = rdK2(J - 2);
        SB();
        if constexpr (hasB) {
          o[1] = mfma32(vf[cu][0], pp, o[1]); pp = expk(sc[cu][1], 1, mB);
          o[1] = mfma32(vf[cu][1], pp, o[1]);
        }
        SB();
      }
      if constexpr (J == 8) {
        wait_vm<0>();
        st32(rws, lane == 0 ? 0u : OOB, f2w, tag);
        SB();
      }
    });
    STAMP(6);
#pragma unroll
    for (int a = 0; a < 2; ++a) {
      float amax = 0.f;
#pragma unroll
      for (int r = 0; r < 12; ++r) amax = __builtin_fmaxf(amax, __builtin_fabsf(o[a][r]));
      const bool okv = (o[a][12] >= 9.0e-13f) & (o[a][12] <= 1.0e12f) & (amax <= 3.0e38f);
      if (!__all(okv)) {
        float mx = -__builtin_inff();
#pragma unroll 1
        for (int pass = 0; pass < 2; ++pass) {
          o[a] = (f32x16)(0.f);
#pragma unroll 1
          for (int j = (jmin > a ? jmin : a); j < 9 + a; ++j) {
            f32x16 s = mfma8(rdK2(j), q2[a], mfma32(rdK1(j), bf8(q1[a]), (f32x16)(0.f)));
            const unsigned fl = j == a ? ~0u : 0u, al = (j == a || j == 8 + a) ? 0u : ~0u;
#pragma unroll
            for (int r = 0; r < 8; ++r) {
              const int m = (int)((mk[r] ^ fl) | al);
              s[2 * r] = (m & 1) ? s[2 * r] : -__builtin_inff();
              s[2 * r + 1] = m < 0 ? s[2 * r + 1] : -__builtin_inff();
            }
            if (pass == 0) {
#pragma unroll
              for (int r = 0; r < 16; ++r) mx = __builtin_fmaxf(mx, s[r]);
            } else {
#pragma unroll
              for (int r = 0; r < 16; ++r) s[r] -= mx;
              o[a] = mfma32(rdV(j, 0), expk(s, 0, 0), o[a]);
              o[a] = mfma32(rdV(j, 1), expk(s, 1, 0), o[a]);
            }
          }
          if (pass == 0) {
            auto t2 = __builtin_amdgcn_permlane32_swap(__float_as_uint(mx), __float_as_uint(mx), false, false);
            mx = __builtin_fmaxf(__uint_as_float(t2[0]), __uint_as_float(t2[1]));
          }
        }
      }
      const float il = __builtin_amdgcn_rcpf(o[a][12]);
      const unsigned so = A2O_OFF + (16 * QB + 4 * rgp + 2 * a) * 20480 + 5 * pair * 256;
      const unsigned vb = (n >> 4) * 20480 + (n & 15) * 16;
      st128(rws, vb + h * 256, so + 3 * e * 256,
            (u32x4){pk2(o[a][0] * il, o[a][1] * il), pk2(o[a][2] * il, o[a][3] * il), pk2(o[a][4] * il, o[a][5] * il), pk2(o[a][6] * il, o[a][7] * il)});
      st64(rws, h ? OOB : vb, so + 512 + 8 * e, (u32x2){pk2(o[a][8] * il, o[a][9] * il), pk2(o[a][10] * il, o[a][11] * il)});
    }
    SB();
    lds_inc(CNTA);
    const u32x4 blast = ld128(rws, l16, B2O_OFF + ct * 102400 + 99 * 1024);
    wait_vm<1>();
    st32(rws, lane == 0 ? 0u : OOB, F3_OFF + (4 * QB + rgp) * 128 + (2 * pair + e) * 4, tag);
    TSTAMP(7);
    unsigned p3 = ld32(rws, n * 4, F3_OFF + rg * 128);
    lds_wait(CNTD, 4);
    TSTAMP(8);
    bf16x8 bo[4][25];
    unsigned ba[4];
#pragma unroll
    for (int P = 0; P < 4; ++P) ba[P] = BO + ((P + ct) & 3) * 25600 + l16;
    auto rdB = [&](int P, int i) {
      bo[P][i] = lds_rd128(ba[P] + i * 1024);
      if (i == 24) bo[P][i] = ((P + ct) & 3) == 3 ? bf8(blast) : bo[P][i];
    };
    static_for<25>([&](auto I) { rdB(0, I); });
    SB();
    if (!__all(p3 == tag)) {
#pragma unroll 1
      for (unsigned sp = 0; sp < SPIN_CAP; ++sp) {
        p3 = ld32(rws, n * 4, F3_OFF + rg * 128);
        if (__all(p3 == tag)) break;
        __builtin_amdgcn_s_sleep(2);
      }
    }
    SB();
    TSTAMP(9);
    u32x4 ao[4][5];
#pragma unroll
    for (int P = 0; P < 4; ++P) {
      const unsigned so = A2O_OFF + (4 * rg + rgp) * 20480 + ((P + ct) & 3) * 5120;
#pragma unroll
      for (int i = 0; i < 5; ++i) ao[P][i] = i < 4 ? ld128(rws, l16 + i * 1024, so) : ld128(rws, l16, so + 4096);
    }
    SB();
    f32x4 acc[5];
    static_for<20>([&](auto II) {
      constexpr int P = II / 5, i = II % 5;
      if constexpr (P < 3) static_for<5>([&](auto C) { rdB(P + 1, 5 * i + C); });
#pragma unroll
      for (int c = 0; c < 5; ++c) acc[c] = mfma16(bo[P][5 * i + c], bf8(ao[P][i]), II == 0 ? (f32x4)(0.f) : acc[c]);
      SB();
    });
    TSTAMP(10);
    const unsigned oo = (64 * rg + 16 * rgp + (lane & 15)) * 1280 + ct * 160 + (lane >> 4) * 8;
    lds_wait(RDYB + rgp * 4, 1);
#pragma unroll
    for (int c = 0; c < 5; ++c) {
      const f32x4 t = acc[c] + lds_rdf4(rgp * 15360 + c * 1024 + l16);
      *(u32x2*)(out + oo + c * 32) = (u32x2){pk2(t[0], t[1]), pk2(t[2], t[3])};
    }
    STAMP(15);
    STAMP_FLUSH(0);
    asm volatile("fused_lx:");
    __builtin_amdgcn_endpgm();
  } else {
    static_rng<52, 80>([&](auto KK) { step(KK, IC(2), IC(1)); });
    epi2();
    wait_l0();
    barrier();
    STAMP(5);
    TAIL_COORDS;
    if (!rdyh) {
#pragma unroll 1
      for (unsigned sp = 0; sp < SPIN_CAP; ++sp) {
        pol1 = polh_ld();
        if (__all(pol1 == tag)) break;
        __builtin_amdgcn_s_sleep(4);
      }
      static_for<16>([&](auto I) { ldA(I / 4, I % 4); });
    }
    asm volatile("" :: "v"(aq[3][3]), "v"(aq[3][2]), "v"(aq[3][1]), "v"(aq[3][0]), "v"(bq[3][4]));
    SB();
    f32x4 acc[4][5];
    auto slot = [&](int s) {
#pragma unroll
      for (int r = 0; r < 4; ++r)
#pragma unroll
        for (int c = 0; c < 5; ++c) acc[r][c] = mfma16(bf8(bq[s][c]), bf8(aq[s][r]), s == 0 ? (f32x4)(0.f) : acc[r][c]);
    };
    const unsigned bsrc = B2O_OFF + ct * 102400 + rgp * 25600, bdst = BO + rgp * 25600;
    auto dmaB = [&](int i) {
      if (i < 6) dma_lin<4>(l16, sdw, bsrc + i * 4096, bdst + i * 4096);
      else dma_lin<1>(l16, sdw, bsrc + (rgp < 3 ? 24576 : 23552), bdst + (rgp < 3 ? 24576 : 23552));
    };
    dmaB(0); dmaB(1); SB(); slot(0); SB();
    static_for<5>([&](auto C) { ldB(4, C); });
    dmaB(2); dmaB(3); SB(); slot(1); SB();
    wait_vm<21>();
    asm volatile("" :: "v"(pfd));
    st32(rws, lane == 0 ? 0u : OOB, f2w, tag);
    static_for<5>([&](auto C) { ldB(5, C); });
    dmaB(4); dmaB(5); dmaB(6); SB(); slot(2); SB();
    static_for<10>([&](auto C) { ldB(6 + C / 5, C % 5); });
    SB(); slot(3); SB();
    STAMP(6);
    unsigned p2 = ld32(rws, (lane & 15) * 4, F2_OFF + rg * 256 + rgp * 64);
    wait_vm<11>();
    lds_inc(CNTD);
    SB();
    if (!__all(p2 == tag)) {
#pragma unroll 1
      for (unsigned sp = 0; sp < SPIN_CAP; ++sp) {
        p2 = ld32(rws, (lane & 15) * 4, F2_OFF + rg * 256 + rgp * 64);
        if (__all(p2 == tag)) break;
        __builtin_amdgcn_s_sleep(2);
      }
    }
    SB();
    TSTAMP(7);
    static_for<12>([&](auto I) { ldA(4 + I / 4, I % 4); });
    SB();
    u32x2 xres[5];
    static_rng<4, 12>([&](auto SS) {
      constexpr int s = SS;
      slot(s); SB();
      if constexpr (s + 3 < 12) static_for<4>([&](auto I) { ldA(s + 3, I); });
      if constexpr (s + 4 < 12) static_for<5>([&](auto C) { ldB(s + 4, C); });
      if constexpr (s == 9) {
        const unsigned oo = (64 * rg + 16 * rgp + (lane & 15)) * 1280 + ct * 160 + (lane >> 4) * 8;
#pragma unroll
        for (int c = 0; c < 5; ++c) xres[c] = *(const u32x2*)(x + oo + c * 32);
      }
      SB();
    });
    TSTAMP(8);
    lds_wait(CNTA, 4);
#pragma unroll
    for (int d = 1; d < 4; ++d) {
      const unsigned a = ((rgp + d) & 3) * 15360 + (d - 1) * 5120 + l16;
#pragma unroll
      for (int c = 0; c < 5; ++c) *(LAS f32x4*)(size_t)(a + c * 1024) = acc[d][c];
    }
    lds_inc(CNTY);
    lds_wait(CNTY, 4);
    TSTAMP(9);
    const unsigned a = rgp * 15360 + l16;
    f32x4 t[5];
#pragma unroll
    for (int c = 0; c < 5; ++c) {
      t[c] = (acc[0][c] + lds_rdf4(a + c * 1024)) + (lds_rdf4(a + 5120 + c * 1024) + lds_rdf4(a + 10240 + c * 1024));
      t[c] += (f32x4){lo_f(xres[c][0]), hi_f(xres[c][0]), lo_f(xres[c][1]), hi_f(xres[c][1])};
    }
    SB();
#pragma unroll
    for (int c = 0; c < 5; ++c) *(LAS f32x4*)(size_t)(a + c * 1024) = t[c];
    asm volatile("" ::: "memory");
    *(volatile LAS unsigned*)(size_t)(RDYB + rgp * 4) = 1u;
    TSTAMP(10);
  }
  STAMP(15);
  STAMP_FLUSH(0);
  asm volatile("fused_ly:");
}

extern "C" void kernel_launch(void* const* d_in, const int* in_sizes, int n_in, void* d_out, int out_size,
                              void* d_ws, size_t ws_size, hipStream_t stream) {
  (void)hipFuncSetAttribute((const void*)fused, hipFuncAttributeMaxDynamicSharedMemorySize, 163840);
  hipLaunchKernelGGL(fused, dim3(256), dim3(512), 163840, stream, (const char*)d_in[0], (const char*)d_in[1],
                     (const char*)d_in[2], (const char*)d_in[4], (const char*)d_in[5], (const char*)d_in[6],
                     (char*)d_ws, (char*)d_out);
}
```

```cpp
#include <hip/hip_runtime.h>
#include <hip/hip_bf16.h>
#include <stdint.h>
#include <utility>

typedef __attribute__((ext_vector_type(2))) __bf16 bf16x2;
typedef __attribute__((ext_vector_type(4))) __bf16 bf16x4;
typedef __attribute__((ext_vector_type(8))) __bf16 bf16x8;
typedef __attribute__((ext_vector_type(4))) short s16x4;
typedef __attribute__((ext_vector_type(2))) float f32x2;
typedef __attribute__((ext_vector_type(4))) float f32x4;
typedef __attribute__((ext_vector_type(16))) float f32x16;
typedef __attribute__((ext_vector_type(2))) unsigned u32x2;
typedef __attribute__((ext_vector_type(4))) unsigned u32x4;
#define LAS __attribute__((address_space(3)))
#define DI __device__ __forceinline__
#define SB() __builtin_amdgcn_sched_barrier(0)
#define IC(v) std::integral_constant<int, (v)>{}

constexpr unsigned QKV_OFF = 0;
constexpr unsigned A2H_OFF = 8388608;
constexpr unsigned A2O_OFF = A2H_OFF + 6291456;
constexpr unsigned B2H_OFF = A2O_OFF + 2621440;
constexpr unsigned B2O_OFF = B2H_OFF + 1966080;
constexpr unsigned CS_OFF = B2O_OFF + 819200;
constexpr unsigned F0_OFF = CS_OFF + 409600;
constexpr unsigned F1_OFF = F0_OFF + 1024;
constexpr unsigned F2_OFF = F1_OFF + 8192;
constexpr unsigned FH_OFF = F2_OFF + 8192;
constexpr unsigned F3_OFF = FH_OFF + 2048;
constexpr unsigned GEN_OFF = F3_OFF + 4096;
constexpr unsigned GR_OFF = GEN_OFF + 1024;
constexpr unsigned WS_END = GR_OFF + 16384;
constexpr unsigned OOB = 0x80000000u;

#define STAMP_DECL
#define STAMP(S)
#define STAMP_FLUSH(K)

#define PSTAMP(S)
#define TSTAMP(S) STAMP(S)
constexpr float LOG2E = 1.4426950408889634f;
constexpr float QSCALE = 0.22360679774997896f * LOG2E;

template <int B, class F, int... I> DI void static_for_impl(F&& f, std::integer_sequence<int, I...>) { (f(std::integral_constant<int, B + I>{}), ...); }
template <int B, int E, class F> DI void static_rng(F&& f) { static_for_impl<B>(f, std::make_integer_sequence<int, E - B>{}); }
template <int N, class F> DI void static_for(F&& f) { static_rng<0, N>(f); }

template <int N> DI void wait_vm() { asm volatile("s_waitcnt vmcnt(%0)" :: "i"(N) : "memory"); }
template <int N, int L> DI void wait_vm_l() { asm volatile("s_waitcnt vmcnt(%0) lgkmcnt(%1)" :: "i"(N), "i"(L) : "memory"); }
template <int N> DI void wait_vm_l0() { asm volatile("s_waitcnt vmcnt(%0) lgkmcnt(0)" :: "i"(N) : "memory"); }
DI void wait_l0() { asm volatile("s_waitcnt lgkmcnt(0)" ::: "memory"); }
DI void barrier() { __builtin_amdgcn_s_barrier(); asm volatile("" ::: "memory"); }

template <unsigned LB> DI void dma_k(unsigned voff, u32x4 srd, unsigned k, unsigned step, unsigned wb) {
  unsigned t;
  asm volatile("s_add_u32 m0, %5, %6\n\ts_mul_i32 %0, %3, %4\n\tbuffer_load_dwordx4 %1, %2, %0 offen lds"
               : "=&s"(t) : "v"(voff), "s"(srd), "s"(k), "s"(step), "s"(wb), "i"(LB) : "memory", "scc");
}
template <unsigned LB> DI void dma_x(unsigned v0, unsigned v1, u32x4 srd, unsigned k, unsigned wb) {
  unsigned t;
  asm volatile("s_add_u32 m0, %5, %6\n\ts_lshl_b32 %0, %4, 7\n\tbuffer_load_dwordx4 %1, %3, %0 offen lds\n\t"
               "buffer_load_dwordx4 %2, %3, %0 offen offset:1024 lds"
               : "=&s"(t) : "v"(v0), "v"(v1), "s"(srd), "s"(k), "s"(wb), "i"(LB) : "memory", "scc");
}
template <int N> DI void dma_lin(unsigned voff, u32x4 srd, unsigned soff, unsigned lds) {
  static_assert(N >= 1 && N <= 4, "");
  if constexpr (N == 1)
    asm volatile("s_mov_b32 m0, %3\n\ts_nop 0\n\tbuffer_load_dwordx4 %0, %1, %2 offen sc1 lds" :: "v"(voff), "s"(srd), "s"(soff), "s"(lds) : "memory");
  if constexpr (N == 2)
    asm volatile("s_mov_b32 m0, %3\n\ts_nop 0\n\tbuffer_load_dwordx4 %0, %1, %2 offen sc1 lds\n\t"
                 "buffer_load_dwordx4 %0, %1, %2 offen offset:1024 sc1 lds" :: "v"(voff), "s"(srd), "s"(soff), "s"(lds) : "memory");
  if constexpr (N == 3)
    asm volatile("s_mov_b32 m0, %3\n\ts_nop 0\n\tbuffer_load_dwordx4 %0, %1, %2 offen sc1 lds\n\t"
                 "buffer_load_dwordx4 %0, %1, %2 offen offset:1024 sc1 lds\n\t"
                 "buffer_load_dwordx4 %0, %1, %2 offen offset:2048 sc1 lds" :: "v"(voff), "s"(srd), "s"(soff), "s"(lds) : "memory");
  if constexpr (N == 4)
    asm volatile("s_mov_b32 m0, %3\n\ts_nop 0\n\tbuffer_load_dwordx4 %0, %1, %2 offen sc1 lds\n\t"
                 "buffer_load_dwordx4 %0, %1, %2 offen offset:1024 sc1 lds\n\t"
                 "buffer_load_dwordx4 %0, %1, %2 offen offset:2048 sc1 lds\n\t"
                 "buffer_load_dwordx4 %0, %1, %2 offen offset:3072 sc1 lds" :: "v"(voff), "s"(srd), "s"(soff), "s"(lds) : "memory");
}
DI void dma_g(unsigned voff, const void* sbase, unsigned lds) {
  asm volatile("s_mov_b32 m0, %2\n\ts_nop 0\n\tglobal_load_lds_dwordx4 %0, %1" :: "v"(voff), "s"(sbase), "s"(lds) : "memory");
}

DI void warm(unsigned& d, unsigned voff, const void* sbase, unsigned long long mask) {
  asm volatile("s_mov_b64 exec, %3\n\tglobal_load_dword %0, %1, %2\n\ts_mov_b64 exec, -1" : "+v"(d) : "v"(voff), "s"(sbase), "s"(mask) : "memory");
}
DI void warm1(unsigned& d, unsigned voff, u32x4 srd, unsigned soff) {
  asm volatile("buffer_load_dword %0, %1, %2, %3 offen sc1" : "+v"(d) : "v"(voff), "s"(srd), "s"(soff) : "memory");
}
DI bf16x8 lds_rd128(unsigned a) { return *(LAS bf16x8*)(size_t)a; }
DI f32x4 lds_rdf4(unsigned a) { return *(LAS f32x4*)(size_t)a; }
DI bf16x4 lds_tr(unsigned a) { return __builtin_amdgcn_ds_read_tr16_b64_v4bf16((LAS bf16x4*)(size_t)a); }
DI bf16x8 cat8(bf16x4 lo, bf16x4 hi) { return __builtin_shufflevector(lo, hi, 0, 1, 2, 3, 4, 5, 6, 7); }
DI bf16x8 lds_trfrag(unsigned a, unsigned step) { return cat8(lds_tr(a), lds_tr(a + step)); }
DI unsigned pk2(float a, float b) {
  f32x2 v = {a, b};
  return __builtin_bit_cast(unsigned, __builtin_convertvector(v, bf16x2));
}
DI float bfr(float a) { return (float)(__bf16)a; }
DI float lo_f(unsigned u) { return __uint_as_float(u << 16); }
DI float hi_f(unsigned u) { return __uint_as_float(u & 0xffff0000u); }
DI bf16x8 bf8(u32x4 v) { return __builtin_bit_cast(bf16x8, v); }
DI f32x16 mfma32(bf16x8 a, bf16x8 b, f32x16 c) { return __builtin_amdgcn_mfma_f32_32x32x16_bf16(a, b, c, 0, 0, 0); }
DI f32x4 mfma16(bf16x8 a, bf16x8 b, f32x4 c) { return __builtin_amdgcn_mfma_f32_16x16x32_bf16(a, b, c, 0, 0, 0); }
DI f32x16 mfma8(u32x2 a, u32x2 b, f32x16 c) {
  return __builtin_amdgcn_mfma_f32_32x32x8bf16_1k(__builtin_bit_cast(s16x4, a), __builtin_bit_cast(s16x4, b), c, 0, 0, 0);
}
typedef __amdgpu_buffer_rsrc_t rsrc_t;
DI rsrc_t mkrsrc(const void* p, unsigned n) { return __builtin_amdgcn_make_buffer_rsrc((void*)p, 0, n, 0x00020000); }
DI void st128(rsrc_t r, unsigned vo, unsigned so, u32x4 v) {
  __builtin_amdgcn_raw_buffer_store_b128(v, r, vo, so, 16);
  asm volatile("s_nop 1" :: "v"(v) : "memory");
}
DI void st64(rsrc_t r, unsigned vo, unsigned so, u32x2 v) { __builtin_amdgcn_raw_buffer_store_b64(v, r, vo, so, 16); }
DI void st32(rsrc_t r, unsigned vo, unsigned so, unsigned v) { __builtin_amdgcn_raw_buffer_store_b32(v, r, vo, so, 16); }
DI u32x4 ld128(rsrc_t r, unsigned vo, unsigned so) { return __builtin_amdgcn_raw_buffer_load_b128(r, vo, so, 16); }
DI u32x2 ld64(rsrc_t r, unsigned vo, unsigned so) { return __builtin_amdgcn_raw_buffer_load_b64(r, vo, so, 16); }
DI unsigned ld32(rsrc_t r, unsigned vo, unsigned so) { return __builtin_amdgcn_raw_buffer_load_b32(r, vo, so, 16); }
DI u32x4 mkdesc(const void* p, unsigned n) {
  const unsigned long long a = (unsigned long long)p;
  return (u32x4){(unsigned)a, (unsigned)(a >> 32) & 0xffffu, n, 0x00020000u};
}
template <int CTRL, int RM> DI float dpp_add(float v) {
  return v + __uint_as_float(__builtin_amdgcn_update_dpp(0, __float_as_uint(v), CTRL, RM, 0xf, true));
}
DI int fresh_lane() {
  int l; asm volatile("v_mbcnt_lo_u32_b32 %0, -1, 0\n\tv_mbcnt_hi_u32_b32 %0, -1, %0" : "=v"(l)); return l;
}
DI int opq(int v) { asm volatile("" : "+s"(v)); return v; }
DI unsigned opq(unsigned v) { asm volatile("" : "+s"(v)); return v; }
DI unsigned vopq(unsigned v) { asm("" : "+v"(v)); return v; }
DI float vmul(float a, float b) { float r; asm("v_mul_f32 %0, %1, %2" : "=v"(r) : "v"(a), "v"(b)); return r; }
DI unsigned vadd(unsigned a, unsigned c) { unsigned r; asm volatile("v_add_u32 %0, %2, %1" : "=v"(r) : "v"(a), "s"(c)); return r; }
DI void lds_inc(unsigned a) {
  asm volatile("s_mov_b64 exec, 1\n\tds_add_u32 %0, %1\n\ts_mov_b64 exec, -1" :: "v"(a), "v"(1u) : "memory");
}
constexpr unsigned SPIN_CAP = 1u << 16;
DI void lds_wait(unsigned a, unsigned v) {
#pragma unroll 1
  for (unsigned sp = 0; sp < 64 * SPIN_CAP; ++sp) {
    if (__builtin_amdgcn_readfirstlane(*(volatile LAS unsigned*)(size_t)a) >= v) break;
    __builtin_amdgcn_s_sleep(1);
  }
  asm volatile("" ::: "memory");
}

constexpr unsigned XOFF = 98304, RTO = 49152 + 8192, EX1 = 49152, EX2 = 51200;
constexpr unsigned xsl(int s) { return XOFF + (unsigned)(s & 3) * 16384u; }
constexpr unsigned wsl1(int s) { return (unsigned)(s & 3) * 24576u; }
constexpr unsigned wsl2(int u) {
  return u == 0 ? 57344u : u == 1 ? 73728u : u == 2 ? 0u : u == 3 ? 16384u : u == 4 ? 32768u :
         u == 5 ? 131072u : u == 6 ? 57344u : u == 7 ? 147456u : u == 8 ? 73728u : 98304u;
}
constexpr unsigned US = 8256, KO = 0, VO = 3 * US;
constexpr unsigned MISC = 61440, CNTA = MISC, CNTY = MISC + 4, CNTD = MISC + 8, RDYB = MISC + 16;
constexpr unsigned ONES = 55424, BO = MISC + 1024;
static_assert(6 * US <= EX2 && BO + 99 * 1024 == 163840 && (ONES & 255) == ((VO + 192) & 255) && ONES >= EX2 + 4096 && ONES + 5120 <= MISC, "lds");

constexpr int K_SL = 1, K_SM = 9, K_CP1 = 14, K_ST0 = 18, K_FL0 = 27, K_GR = 26, K_CS1 = 26, K_CF1 = 33, K_PL0 = 42, K_CK0 = 50, K_FL1 = 51, K_FH1 = 59;
constexpr int K_CP2 = 49, K_PL1 = 58, K_CS2 = 56, K_CH1 = 65, K_CH2 = 73, K_BH = 58, K_PH1 = 64, K_AH = 70, K_ROLE = 52;
struct Ops { short id[320]; short k[320]; short dma[320]; int n; };
constexpr int p2_at(int K) {
  return K == 31 ? 0 : K == 32 ? 1 : K == 35 ? 2 : K == 36 ? 3 : K == 39 ? 4 : K == 40 ? 5 : K == 42 ? 6 : K == 43 ? 7 :
         K == 44 ? 8 : K == 46 ? 9 : K == 47 ? 10 : K == 48 ? 11 : (K >= 51 && K <= 58) ? K - 39 : -1;
}
constexpr Ops make_ops(bool odd, bool y) {
  Ops o{}; int n = 0;
  auto add = [&](int id, int k, int d) { o.id[n] = (short)id; o.k[n] = (short)k; o.dma[n] = (short)d; ++n; };
  for (int i = 0; i < 5; ++i) add(100 + i, -3, 1);
  add(4, -3, 0); add(2, -3, 0);
  for (int i = 5; i < 10; ++i) add(100 + i, -3, 1);
  for (int K = 0; K < 64; ++K) {
    if (K >= 3 && K <= 30) {
      const int s = ((K - 3) >> 2) + 3, rel = (K - 3) & 3;
      if (rel == 0) { add(100 + 5 * s, K, 1); add(101 + 5 * s, K, 1); } else add(101 + 5 * s + rel, K, 1);
    }
    if (p2_at(K) >= 0) add(200 + p2_at(K), K, 1);
    if (K == 0) { add(110, K, 0); add(111, K, 0); }
    if (K == 1) add(112, K, 0);
    if (K == 2) { add(113, K, 0); add(114, K, 0); }
    if (K == K_SL) for (int i = 0; i < 4; ++i) add(3, K, 0);
    if (K == K_CP1 || K == K_CP2) add(332, K, 0);
    if (K == K_ST0) { add(310, K, 0); add(310, K, 0); }
    if (K == K_GR) add(320, K, 0);
    if (K == K_CS1) { add(325, K, 2); add(325, K, 2); add(325, K, 2); }
    if (K == K_PL0) add(340, K, 2);
    if (K >= 40 && K < (odd ? 46 : 42)) add(330, K, 0);
    if (!odd && (K == 45 || K == 49)) add(331, K, 0);
    if (!y && K == K_CS2) { add(350, K, 0); add(350, K, 0); }
    if (!y && K == K_CS2 + 1) add(350, K, 0);
    if (!y && K == K_PL1) add(350, K, 0);
    if (y && K >= K_BH) add(350, K, 0);
  }
  o.n = n; return o;
}
constexpr Ops OPS[4] = {make_ops(false, false), make_ops(true, false), make_ops(false, true), make_ops(true, true)};
constexpr bool stage_has(int id, int S) {
  return (id >= 100 && id < 200) ? (id - 100) / 5 <= S : (id >= 200 && id < 300) ? (id - 200) / 2 + 10 <= S : false;
}
constexpr int cmin(int a, int b) { return a < b ? a : b; }
constexpr int clampw(int w) { return w < 0 ? 0 : (w > 63 ? 63 : w); }
constexpr int wait_bar(const Ops& o, int K) {
  const int S = K == 63 ? 17 : (K >> 2) + 1;
  int last = -1, c = 0;
  for (int i = 0; i < o.n; ++i)
    if (stage_has(o.id[i], S) || (K == K_FL0 && o.id[i] == 310) || (K == K_FL1 && o.id[i] == 330) || (K == K_FH1 && o.id[i] == 331)) last = i;
  for (int i = last + 1; i < o.n; ++i) if (o.k[i] < K && o.dma[i] != 2) ++c;
  return clampw(c);
}
constexpr int wait_mid(const Ops& o, int K, int id) {
  int last = -1, c = 0;
  for (int i = 0; i < o.n; ++i) if (o.id[i] == id) last = i;
  for (int i = last + 1; i < o.n; ++i) if ((o.k[i] < K && o.dma[i] != 2) || (o.k[i] == K && o.dma[i] == 1)) ++c;
  return clampw(c);
}

__global__ __launch_bounds__(512, 2) void fused(const char* __restrict__ x, const char* __restrict__ wqkv,
                                                const char* __restrict__ wo, const char* __restrict__ wg,
                                                const char* __restrict__ wu, const char* __restrict__ wd,
                                                char* __restrict__ ws, char* __restrict__ out) {
  const int b = blockIdx.x;
  const int mt = 8 * (b & 1) + (b >> 5), g = 4 * ((b >> 1) & 3) + ((b >> 3) & 3), odd = g & 1, p = g >> 1;
  const int w = __builtin_amdgcn_readfirstlane(threadIdx.x >> 6);
  const int rgp = w & 3, nlo = w >> 2;
  int lane = threadIdx.x & 63, h = lane >> 5, n = lane & 31;
  unsigned l16 = lane * 16;
#define REFRESH() do { lane = fresh_lane(); h = lane >> 5; n = lane & 31; l16 = lane * 16; } while (0)
#define KB0 42
#define KB1 46
#define KB2 50
#define ROT 0
  const int ag = (0x8530 >> (4 * (g & 3))) & 15;
  const int own = (mt & 7) + 2 * (mt >> 3), pos = (mt < 8 || mt >= 14) ? 0 : 5;
  const int r0 = (own + ag + 2 * (g >> 2)) % 10;
  STAMP_DECL; STAMP(0);

  unsigned pfd = 0;
  const unsigned wb1 = w * 1024, wb2 = w * 2048;
  const u32x4 sdx = mkdesc(x + (size_t)(128 * mt + 16 * w) * 1280, 0x40000000u);
  const u32x4 sdq = mkdesc(wqkv + (size_t)(8 * w) * 3840, 0x40000000u);
  const u32x4 sd1 = mkdesc(odd ? wqkv + (size_t)(8 * w) * 3840 : wg + (size_t)(8 * w) * 3072, 0x40000000u);
  const u32x4 sd2 = mkdesc(odd ? wqkv + (size_t)(8 * w) * 3840 : wu + (size_t)(8 * w) * 3072, 0x40000000u);
  const unsigned step12 = odd ? 245760u : 196608u;
  unsigned xv0, xv1, wv[3];
  {
    const int r = lane >> 3, u = lane & 7, sw = ((r >> 1) & 1) << 2;
    xv0 = r * 1280 + ((u ^ (r >> 1)) << 4);
    xv1 = (r + 8) * 1280 + ((u ^ (4 + (r >> 1))) << 4) - 1024;
    const unsigned vq = r * 3840 + ((u ^ sw) << 4);
    wv[0] = vq + (4 * p + odd) * 128;
    wv[1] = odd ? vq + (p == 7 ? 29 : 4 * p + 2) * 128 : r * 3072 + ((u ^ sw) << 4) + 3 * p * 128;
    wv[2] = odd ? vq + (p == 7 ? 29 : 4 * p + 3) * 128 : r * 3072 + ((u ^ sw ^ 2) << 4) + 3 * p * 128;
  }
  auto kst = [&](int pos) { int k = r0 + pos; return (unsigned)(k >= 10 ? k - 10 : k); };
  auto issue1 = [&](auto SS, auto RR) {
    constexpr int s = SS, rel = RR;
    if constexpr (rel == 0) dma_x<xsl(s)>(xv0, xv1, sdx, kst(s), wb2);
    else dma_k<wsl1(s) + (rel - 1) * 8192>(wv[rel - 1], rel == 1 ? sdq : rel == 2 ? sd1 : sd2, kst(s), rel == 1 ? 245760u : step12, wb1);
  };
  PSTAMP(7);
  static_for<4>([&](auto I) { issue1(IC(0), I); });
  SB();
  PSTAMP(8);
  barrier();
  STAMP(1);
  {
    unsigned long long pc; unsigned len, l2;
    asm volatile("s_getpc_b64 %0\n1:\ts_mov_b32 %1, fused_lx-1b\n\ts_mov_b32 %2, fused_ly-1b\n\ts_max_u32 %1, %1, %2"
                 : "=s"(pc), "=s"(len), "=s"(l2) :: "scc");
    const u32x4 sdc = {(unsigned)pc, (unsigned)(pc >> 32) & 0xffffu, len, 0x00020000u};
    asm volatile("buffer_load_dword %0, %1, %2, %3 offen" : "+v"(pfd) : "v"(lane < 2 ? lane * 128 : OOB), "s"(sdc), "s"(((b >> 3) * 8 + w) * 256) : "memory");
  }
  u32x4 sx4; unsigned sx1, gen;
  const int srow = 128 * mt + 8 * g + w;
  asm volatile("global_load_dword %0, %1, %2 sc1" : "=v"(gen) : "v"(0), "s"(ws + GEN_OFF) : "memory");
  static_for<4>([&](auto I) { issue1(IC(1), I); });
  const int jok = b < 170, jb = jok ? b : b - 170;
  const int jG = (jb * 241) >> 12, jks = (jb - 17 * jG) * 4 + (w >> 1);
  SB();
  PSTAMP(9);

  unsigned wa1[3], wa2[2], xa[4];
  {
    const int cb = (lane >> 4) & 1, q = (lane >> 2) & 3, pl = (lane >> 1) & 1, ph = lane & 1;
    const unsigned rowoff = (8 * h + q) * 128 + pl * 8;
    const int sw = (q >> 1) << 2;
#pragma unroll
    for (int i = 0; i < 3; ++i) {
      const int j = 3 * nlo + i;
      const int piece = odd ? (j >> 1) : 0, sel = odd ? (j & 1) : nlo, t = 2 * nlo + i - 1;
      const unsigned aq = piece * 8192 + (((4 * sel + 2 * cb + ph) ^ sw) << 4);
      const unsigned ag = (1 + cb) * 8192 + (((2 * t + ph) ^ sw ^ (cb << 1)) << 4);
      wa1[i] = rowoff + ((odd || i == 0) ? aq : ag);
    }
#pragma unroll
    for (int i = 0; i < 2; ++i) wa2[i] = rowoff + cb * 8192 + (((2 * (2 * nlo + i) + ph) ^ sw ^ (cb << 1)) << 4);
    const int row = 32 * rgp + n, sx = (row >> 1) & 7;
#pragma unroll
    for (int j = 0; j < 4; ++j) xa[j] = XOFF + row * 128 + (((2 * j + h) ^ sx) << 4);
  }
  auto mksel = [&]() {
    const int fl = fresh_lane(), i = fl & 15, kg = fl >> 4;
    const unsigned o1 = ((i & 3) | (((i >> 2) ^ kg) & 1)) == 0 ? 0x3f803f80u : 0u;
    return bf8((u32x4){o1, o1, o1, o1});
  };
  const rsrc_t rws = mkrsrc(ws, WS_END);
  const u32x4 sdw = mkdesc(ws, WS_END);
  SB();
  PSTAMP(10);
  wait_vm<7>();
  PSTAMP(14);
  barrier();
  STAMP(2);

  f32x16 acc1[3], acc2[2];
  f32x4 cs = (f32x4)(0.f);
  bf16x8 xr[56];
  bf16x8 wf[2][3], csf[4];
  unsigned wcs = rgp == 0 ? vopq(wa1[0]) : rgp == 1 ? vopq(wa1[1]) : vopq(wa1[2]);
  unsigned wcur[3];
  bf16x8 rt[2];
  u32x2 grn;
  u32x4 cl[3];
  unsigned tag, nm;
  float sp0, ss1, ss2;
  bf16x8 acs[3];
  float rstd, sdv, gsc;
  u32x4 pol, hq, sdg, sdu;
  unsigned pol1, st1 = 0, rdyh = 0, vs, vh, gv2, uv2;
  u32x4 q1[2]; u32x2 q2[2];
  u32x4 bq[12][5], aq[12][4];

  auto rd = [&](auto KK) {
    constexpr int K = KK;
    if constexpr (K < 40) {
      constexpr int s = K >> 2, j = K & 3;
      xr[K] = lds_rd128(xa[j] + (xsl(s) - XOFF));
      if constexpr (j == 0 && (s & 3) == 3) {
#pragma unroll
        for (int i = 0; i < 3; ++i) wcur[i] = vadd(wa1[i], wsl1(s));
      }
#pragma unroll
      for (int i = 0; i < 3; ++i) wf[K & 1][i] = lds_trfrag(((s & 3) == 3 ? wcur[i] : wa1[i] + wsl1(s)) + j * 2048, 512);
    } else if constexpr (K < 80) {
      constexpr int u = (K - 40) >> 2, j = K & 3;
      if constexpr (u < 4) xr[K] = lds_rd128(xa[j] + (xsl(6 + u) - XOFF));
      constexpr bool far = wsl2(u) > 57344;
      if constexpr (j == 0 && far) {
#pragma unroll
        for (int i = 0; i < 2; ++i) wcur[i] = vadd(wa2[i], wsl2(u));
      }
#pragma unroll
      for (int i = 0; i < 2; ++i) wf[K & 1][i] = lds_trfrag((far ? wcur[i] : wa2[i] + wsl2(u)) + j * 2048, 512);
    }
  };
  auto cs_ok = [&]() {
    return (((cl[0][1] ^ tag) | (cl[0][3] ^ tag)) | ((cl[1][1] ^ tag) | (cl[1][3] ^ tag)) | (((cl[2][1] ^ tag) | (cl[2][3] ^ tag)) & (unsigned)(h - 1))) == 0;
  };
  auto cs_fin = [&](unsigned src, unsigned dst) {
    if (!__all(cs_ok())) {
#pragma unroll 1
      for (unsigned sp = 0; sp < SPIN_CAP; ++sp) {
        __builtin_amdgcn_s_sleep(4);
        cl[0] = ld128(rws, l16, src); cl[1] = ld128(rws, l16 + 1024, src); cl[2] = ld128(rws, h ? OOB : l16, src + 2048);
        if (__all(cs_ok())) break;
      }
    }
    const float c = ((__uint_as_float(cl[0][0]) + __uint_as_float(cl[0][2])) + (__uint_as_float(cl[1][0]) + __uint_as_float(cl[1][2]))) +
                    (__uint_as_float(cl[2][0]) + __uint_as_float(cl[2][2]));
    const unsigned u0 = __float_as_uint(c);
    const float r1 = c - __uint_as_float(u0 & 0xffff0000u);
    const unsigned u1 = __float_as_uint(r1);
    const float r2 = r1 - __uint_as_float(u1 & 0xffff0000u);
    *(LAS u32x4*)(size_t)(dst + l16) = (u32x4){__builtin_amdgcn_perm(u1, u0, 0x07060302u), __float_as_uint(r2) >> 16, 0u, 0u};
  };
  auto qkv_st = [&](const f32x16& a16, int cu0, int a) {
    const float r = rstd * (cu0 < 80 ? QSCALE : 1.f);
    u32x4 val;
#pragma unroll
    for (int i = 0; i < 4; ++i) val[i] = pk2(vmul(a16[8 * a + 2 * i], r), vmul(a16[8 * a + 2 * i + 1], r));
    st128(rws, vs, QKV_OFF + (cu0 + 2 * a) * 32768 + (128 * mt + 32 * rgp) * 16, val);
  };
  auto gu2 = [&](const f32x16& a16, int i) {
    float t0, t1; unsigned r;
    asm("v_mul_f32 %1, %3, %7\n\tv_mul_f32 %2, %4, %7\n\tv_exp_f32 %1, %1\n\tv_exp_f32 %2, %2\n\t"
        "v_fma_f32 %1, %1, %8, %8\n\tv_fma_f32 %2, %2, %8, %8\n\tv_rcp_f32 %1, %1\n\tv_rcp_f32 %2, %2\n\t"
        "v_mul_f32 %1, %5, %1\n\tv_mul_f32 %2, %6, %2\n\tv_cvt_pk_bf16_f32 %0, %1, %2"
        : "=v"(r), "=&v"(t0), "=&v"(t1)
        : "v"(a16[2 * i]), "v"(a16[2 * i + 1]), "v"(a16[8 + 2 * i]), "v"(a16[9 + 2 * i]), "v"(gsc), "v"(sdv));
    hq[i] = r;
  };
  auto gu_st = [&](int unit, int t) {
    st128(rws, vh, A2H_OFF + (((2 * mt + (rgp >> 1)) * 48 + 2 * unit + (t >> 1)) * 4 + 2 * (rgp & 1)) * 1024 + (t & 1) * 512, hq);
  };
  auto kv_piece = [&](int c) {
    const int bb = opq(b), pair = 2 * (bb & 7) + ((bb >> 4) & 1), e = (bb >> 3) & 1, QB = bb >> 5;
    int t0 = 256 * QB - 256 + 128 * rgp; t0 = t0 < 0 ? 0 : t0;
    if (c < 6) dma_lin<2>(l16, sdw, QKV_OFF + (80 + 80 * (c / 3) + 5 * pair + 2 * e + c % 3) * 32768 + t0 * 16, c * US + rgp * 2048);
    else {
      const unsigned so = QKV_OFF + 5 * pair * 32768 + (256 * QB + 64 * rgp + 32 * (c - 6)) * 16;
      q1[c - 6] = ld128(rws, vs, so + 3 * e * 32768);
      q2[c - 6] = ld64(rws, h ? OOB : n * 16, so + 2 * 32768 + 8 * e);
    }
  };
  auto pol1_on = [&]() { return (lane < 24) & (lane >= (opq(b) >= 32 ? 0 : 12)); };
  auto pol1_ld = [&]() {
    const int bb = opq(b);
    return ld32(rws, pol1_on() ? lane * 4 : OOB, F1_OFF + ((bb & 7) * 8 + (bb >> 5)) * 128);
  };
  auto pol1_ok = [&]() { return __all((pol1 == tag) | !pol1_on()); };
  unsigned av[4], l16b, kt1, kt2, abase, bbase, soa, sob;
  auto ksof = [&](int s) { return ((s < 4 ? opq(kt1) >> (4 * s) : opq(kt2) >> (4 * (s - 4))) & 15u); };
  auto ldB = [&](int s, int c) {
    if (c == 0) sob = bbase + ksof(s) * 5120;
    bq[s][c] = c < 4 ? ld128(rws, l16 + c * 1024, sob) : ld128(rws, l16b, sob);
  };
  auto ldA = [&](int s, int r) {
    if (r == 0) soa = abase + ksof(s) * 4096;
    aq[s][r] = ld128(rws, av[r], soa);
  };
  auto polh_ld = [&]() {
    const int bb = opq(b);
    return ld32(rws, (lane & 1) * 4, FH_OFF + (2 * (bb & 7) + (bb >> 7)) * 128 + rgp * 8);
  };

  auto step = [&](auto KK, auto OD, auto RL) {
    constexpr int K = KK, j = K & 3, ODD = OD, ROLE = RL;
    if constexpr (K == 40) STAMP(3);
    if constexpr (j == 3 && K <= 63) {
      constexpr int nx = ODD < 2 ? wait_bar(OPS[ODD & 1], K) : cmin(wait_bar(OPS[0], K), wait_bar(OPS[1], K));
      constexpr int ny = ODD < 2 ? wait_bar(OPS[2 + (ODD & 1)], K) : cmin(wait_bar(OPS[2], K), wait_bar(OPS[3], K));
      static_assert(ROLE < 2 || nx == ny, "");
      if constexpr (K == 35) wait_vm_l<nx, 7>(); else wait_vm<(ROLE == 1 ? ny : nx)>();
      barrier();
      if constexpr (K == K_FL0) { if (w == 0) st32(rws, fresh_lane() == 0 ? 0u : OOB, F0_OFF + b * 4, tag); }
      if constexpr (K == K_FL1) {
        if (w == 0) {
          const int l = fresh_lane(), ui = l >> 2, hi = (l >> 1) & 1, qi = l & 1;
          const int u = 4 * p + odd + ui;
          const int G = ((u * 4 * 205) >> 10) + hi;
          const int fu = (5 * G) >> 2;
          const int qb = (mt >> 1) + qi;
          const bool on = (ui < (odd ? 3 : 1)) & (u < 30) & (fu <= u) & (qb < 8);
          const unsigned a = ((G & 7) * 8 + qb) * 128 + ((mt - 2 * qb + 2) * 6 + (G >> 3) * 2 + (u - fu)) * 4;
          st32(rws, on ? a : OOB, F1_OFF, tag);
        }
      }
      if constexpr (K == K_FH1 && ROLE == 0) { if (w == 0 && !odd) st32(rws, fresh_lane() == 0 ? 0u : OOB, FH_OFF + mt * 128 + p * 4, tag); }
    }
    if constexpr (K == 71) { asm volatile("s_waitcnt lgkmcnt(4)" ::: "memory"); barrier(); }
    rd(IC(K + 1));
    if constexpr (K >= 3 && K <= 30) issue1(IC(((K - 3) >> 2) + 3), IC((K - 3) & 3));
    if constexpr (p2_at(K) >= 0) {
      constexpr int u = p2_at(K) >> 1, jj = p2_at(K) & 1;
      dma_k<wsl2(u) + jj * 8192>(jj ? uv2 : gv2, jj ? sdu : sdg, kst((u + 6) % 10), 196608u, wb1);
    }
    if constexpr (K < 40) {
#pragma unroll
      for (int i = 0; i < 3; ++i) acc1[i] = mfma32(wf[K & 1][i], xr[K], K == 0 ? (f32x16)(0.f) : acc1[i]);
      __builtin_amdgcn_sched_group_barrier(0x8, 1, 0); __builtin_amdgcn_sched_group_barrier(0x100, 3, 0);
      __builtin_amdgcn_sched_group_barrier(0x8, 1, 0); __builtin_amdgcn_sched_group_barrier(0x100, 2, 0);
      __builtin_amdgcn_sched_group_barrier(0x8, 1, 0); __builtin_amdgcn_sched_group_barrier(0x100, 2, 0);
    } else {
      constexpr int u = (K - 40) >> 2;
#pragma unroll
      for (int i = 0; i < 2; ++i) acc2[i] = mfma32(wf[K & 1][i], xr[u < 4 ? K : K - 56], K == 40 ? (f32x16)(0.f) : acc2[i]);
      __builtin_amdgcn_sched_group_barrier(0x8, 1, 0); __builtin_amdgcn_sched_group_barrier(0x100, 3, 0);
      __builtin_amdgcn_sched_group_barrier(0x8, 1, 0); __builtin_amdgcn_sched_group_barrier(0x100, 5, 0);
    }
    SB();
    if constexpr (K < 40) asm volatile("" :: "v"(acc1[0]), "v"(acc1[1]), "v"(acc1[2]));
    else asm volatile("" :: "v"(acc2[0]), "v"(acc2[1]));
    if constexpr (K == 0 || K == 3 || K == 7 || K == 11 || K == 15) {
      if (pos == (K + 1) >> 2) {
#pragma unroll
        for (int i = 0; i < 4; ++i) csf[i] = lds_trfrag(wcs + wsl1((K + 1) >> 2) + i * 2048, 512);
      }
    }
    if constexpr (K == 47) {
      const unsigned a = vadd(wcs, pos < 2 ? wsl2(0) + pos * 16384 : (pos - 2) * 16384);
      if (pos < 5) {
#pragma unroll
        for (int i = 0; i < 4; ++i) csf[i] = lds_trfrag(a + i * 2048, 512);
      }
    }
    if constexpr (K == 1 || K == 4 || K == 8 || K == 12 || K == 16 || K == 48) {
      if (K == 48 ? pos < 5 : pos == K >> 2) {
        const bf16x8 selv = mksel();
        cs = (f32x4)(0.f);
#pragma unroll
        for (int i = 0; i < 4; ++i) cs = mfma16(selv, csf[i], cs);
      }
    }
    if constexpr (K == 0) issue1(IC(2), IC(0));
    if constexpr (K == 1) issue1(IC(2), IC(1));
    if constexpr (K == 2) { issue1(IC(2), IC(2)); issue1(IC(2), IC(3)); }
    if constexpr (K == K_SL) {
      const char* rp = x + (size_t)srow * 1280;
      asm volatile("global_load_dwordx4 %0, %1, %2" : "=v"(sx4) : "v"(lane * 16), "s"(rp) : "memory");
      asm volatile("global_load_dword %0, %1, %2 offset:1024" : "=v"(sx1) : "v"(lane * 4), "s"(rp) : "memory");
      const char* src = (jks < 48 ? wd + jks * (32 * 1280) : wo + (jks - 48) * (32 * 1280)) + (w & 1) * (16 * 1280) + jG * 128;
      const unsigned vo = (lane >> 3) * 1280 + (lane & 7) * 16;
      asm volatile("global_load_dwordx4 %0, %1, %2" : "=v"(rt[0]) : "v"(vo), "s"(src) : "memory");
      asm volatile("global_load_dwordx4 %0, %1, %2" : "=v"(rt[1]) : "v"(vo), "s"(src + 8 * 1280) : "memory");
    }
    if constexpr (K == K_SM) {
      asm volatile("s_waitcnt vmcnt(%5)" : "+v"(sx4), "+v"(sx1), "+v"(gen), "+v"(rt[0]), "+v"(rt[1]) : "i"(wait_mid(OPS[0], K, 3)) : "memory");
      tag = __builtin_amdgcn_readfirstlane(gen) * 0x9E3779B1u + 0x7F4A7C15u;
      sp0 = lo_f(__builtin_amdgcn_readfirstlane(sx4[0]));
      ss1 = 0.f; ss2 = 0.f;
    }
    if constexpr (K == K_SM || K == K_SM + 1) {
#pragma unroll
      for (int i = K == K_SM ? 0 : 2; i < (K == K_SM ? 2 : 5); ++i) {
        const unsigned u = i < 4 ? sx4[i] : sx1;
        const float d0 = lo_f(u) - sp0, d1 = hi_f(u) - sp0;
        ss1 += d0 + d1;
        ss2 = __builtin_fmaf(d0, d0, ss2); ss2 = __builtin_fmaf(d1, d1, ss2);
      }
    }
    if constexpr (K == K_SM + 3) {
      ss1 = dpp_add<0x111, 15>(ss1); ss2 = dpp_add<0x111, 15>(ss2); ss1 = dpp_add<0x112, 15>(ss1); ss2 = dpp_add<0x112, 15>(ss2);
      ss1 = dpp_add<0x114, 15>(ss1); ss2 = dpp_add<0x114, 15>(ss2); ss1 = dpp_add<0x118, 15>(ss1); ss2 = dpp_add<0x118, 15>(ss2);
      ss1 = dpp_add<0x142, 10>(ss1); ss2 = dpp_add<0x142, 10>(ss2); ss1 = dpp_add<0x143, 12>(ss1); ss2 = dpp_add<0x143, 12>(ss2);
    }
    if constexpr (K == K_SM + 4) {
      const float md = ss1 * (1.f / 640.f);
      const float var = __builtin_fmaxf(__builtin_fmaf(ss2, 1.f / 640.f, -md * md), 0.f);
      grn[0] = pk2(sp0 + md, __builtin_amdgcn_sqrtf(bfr(bfr(var) + 1e-5f)));
    }
    if constexpr (K == K_CP1 || K == K_CP2) {
      REFRESH();
      const bool on = lane < ((rgp < (K == K_CP2 ? 2 : 3) && pos < 5) ? 32 : 0);
      unsigned vo = on ? n * 16 : OOB, d = __float_as_uint(cs[0]);
      const unsigned so = CS_OFF + (g * 10 + (K == K_CP2 ? 6 + 2 * nlo : 3 * nlo) + rgp) * 2560 + (own >> 1) * 512 + (own & 1) * 8;
      if constexpr (K == K_CP1) { vo = lane == 63 ? GR_OFF + srow * 8 - so : vo; d = lane == 63 ? grn[0] : d; }
      st64(rws, vo, so, (u32x2){d, tag});
    }
    if constexpr (K == 15) {
      *(LAS bf16x8*)(size_t)(RTO + wb1 + l16) = rt[0];
      *(LAS bf16x8*)(size_t)(RTO + 8192 + wb1 + l16) = rt[1];
    }
    if constexpr (K == 16) {
      asm volatile("" ::: "memory");
      const unsigned a = RTO + wb1 + ((lane >> 2) & 3) * 128 + (lane >> 4) * 32 + (lane & 3) * 8;
      rt[0] = lds_trfrag(a, 512); rt[1] = lds_trfrag(a + 8192, 512);
      asm volatile("" : "+v"(rt[0]), "+v"(rt[1]) :: "memory");
      const int f = 4 * jG + (lane >> 4), ct = (f * 205) >> 10, c = f - 5 * ct;
      vs = jok ? (ct * (jks < 48 ? 240 : 100) + c) * 1024 + (lane & 15) * 16 : OOB;
    }
    if constexpr (K == K_ST0) {
      const unsigned so = (jks < 48 ? B2H_OFF + jks * 5120 : B2O_OFF + (jks - 48) * 5120) + (w & 1) * 512;
      st128(rws, vs, so, __builtin_bit_cast(u32x4, rt[0]));
      st128(rws, vs, so + 256, __builtin_bit_cast(u32x4, rt[1]));
    }
    if constexpr (K == 21) {
      REFRESH();
      const int r = lane >> 3, u = lane & 7, sw = ((r >> 1) & 1) << 2;
      gv2 = r * 3072 + ((u ^ sw) << 4) + (3 * p + 1 + odd) * 128;
      uv2 = r * 3072 + ((u ^ sw ^ 2) << 4) + (3 * p + 1 + odd) * 128;
    }
    if constexpr (K == 28) sdg = mkdesc(wg + (size_t)(8 * opq(w)) * 3072, 0x40000000u);
    if constexpr (K == 29) sdu = mkdesc(wu + (size_t)(8 * opq(w)) * 3072, 0x40000000u);
    if constexpr (K == K_GR)
      asm volatile("buffer_load_dwordx2 %0, %1, %2, %3 offen sc1" : "=v"(grn) : "v"(n * 8), "s"(sdw), "s"(GR_OFF + (128 * mt + 32 * rgp) * 8) : "memory");
    if constexpr (K == K_CS1) {
      if (w < 6)
        asm volatile("buffer_load_dwordx4 %0, %3, %5, %6 offen sc1\n\tbuffer_load_dwordx4 %1, %3, %5, %6 offen offset:1024 sc1\n\t"
                     "buffer_load_dwordx4 %2, %4, %5, %6 offen offset:2048 sc1"
                     : "=&v"(cl[0]), "=&v"(cl[1]), "=&v"(cl[2]) : "v"(l16), "v"(h ? OOB : l16), "s"(sdw), "s"(CS_OFF + (g * 10 + w) * 2560) : "memory");
    }
    if constexpr (K == K_CF1) {
      asm volatile("s_waitcnt vmcnt(%4)" : "+v"(grn), "+v"(cl[0]), "+v"(cl[1]), "+v"(cl[2]) : "i"(wait_mid(OPS[0], K, 325)) : "memory");
      if (w < 6) cs_fin(CS_OFF + (g * 10 + w) * 2560, EX1 + wb1);
    }
    if constexpr (K == K_PL0) {
      if (w == 7) asm volatile("buffer_load_dwordx4 %0, %1, %2, %3 offen sc1" : "=v"(pol) : "v"(l16), "s"(sdw), "s"(F0_OFF) : "memory");
    }
    if constexpr (K == K_CF1 + 1) {
      if (!__all(grn[1] == tag)) {
#pragma unroll 1
        for (unsigned sp = 0; sp < SPIN_CAP; ++sp) {
          __builtin_amdgcn_s_sleep(4);
          grn = ld64(rws, n * 8, GR_OFF + (128 * mt + 32 * rgp) * 8);
          if (__all(grn[1] == tag)) break;
        }
      }
      nm = ((grn[0] & 0xffffu) ^ 0x8000u) * 0x10001u;
      sdv = hi_f(grn[0]);
      rstd = __builtin_amdgcn_rcpf(sdv);
      gsc = -LOG2E * rstd;
    }
    if constexpr (K == 35) {
#pragma unroll
      for (int i = 0; i < 3; ++i) acs[i] = lds_rd128(EX1 + (3 * nlo + i) * 1024 + l16);
    }
    if constexpr (K >= 36 && K < 39) acc1[K - 36] = mfma32(acs[K - 36], bf8((u32x4){nm, nm, nm, nm}), acc1[K - 36]);
    if constexpr (K == 38) wcs = (rgp & 1) ? vopq(wa2[1]) : vopq(wa2[0]);
    if constexpr (K == K_CK0) {
      if (w == 7) {
        asm volatile("s_waitcnt vmcnt(%1)" : "+v"(pol) : "i"(cmin(wait_mid(OPS[0], K, 340), wait_mid(OPS[1], K, 340))) : "memory");
        auto ok = [&]() { return ((pol[0] ^ tag) | (pol[1] ^ tag) | (pol[2] ^ tag) | (pol[3] ^ tag)) == 0; };
        if (!__all(ok())) {
#pragma unroll 1
          for (unsigned sp = 0; sp < SPIN_CAP; ++sp) {
            __builtin_amdgcn_s_sleep(4);
            pol = ld128(rws, fresh_lane() * 16, F0_OFF);
            if (__all(ok())) break;
          }
        }
        if (b == 0) st32(rws, fresh_lane() == 0 ? 0u : OOB, GEN_OFF, tag);
      }
    }
    if constexpr (K == 39) {
      REFRESH();
      vs = h * 32768 + n * 16;
      vh = (n >> 4) * 1024 + h * 256 + (n & 15) * 16;
    }
    if constexpr (K == 40 || K == 41) qkv_st(acc1[0], odd ? 32 * p + 8 + 12 * nlo : 32 * p + 4 * nlo, K & 1);
    if constexpr (K >= 42 && K < 50) {
      constexpr int z = K - 42;
      if (odd) {
        if constexpr (K < 46) qkv_st(acc1[(K - 40) >> 1], 32 * p + 8 + 12 * nlo + 4 * ((K - 40) >> 1), K & 1);
      } else {
        gu2(acc1[1 + (z >> 2)], z & 3);
        if constexpr ((z & 3) == 3) gu_st(3 * p, 2 * nlo + (z >> 2));
      }
    }
    if constexpr (K == K_ROLE) {
      REFRESH();
      if constexpr (ROLE == 1) {
#pragma unroll
        for (int r = 0; r < 4; ++r) av[r] = l16 + ((r + rgp) & 3) * 1024;
        l16b = l16 + 4096;
        const int bb = opq(b), ct = (bb >> 3) & 7;
        kt1 = 0x76107610u >> (4 * (ct & 3));
        kt2 = (unsigned)(0xBA985432BA985432ull >> (4 * ((ct + 2 * (bb >> 6)) & 7)));
        abase = A2H_OFF + ((4 * (bb & 7) + (bb >> 6)) * 48 + 12 * rgp) * 4096;
        bbase = B2H_OFF + (ct * 48 + 12 * rgp) * 5120;
      }
    }
    if constexpr (K > K_ROLE) {
      if constexpr (ROLE == 0) {
        if constexpr (K == K_CS2) { cl[0] = ld128(rws, l16, CS_OFF + (g * 10 + 6 + w) * 2560); cl[1] = ld128(rws, l16 + 1024, CS_OFF + (g * 10 + 6 + w) * 2560); }
        if constexpr (K == K_CS2 + 1) cl[2] = ld128(rws, h ? OOB : l16, CS_OFF + (g * 10 + 6 + w) * 2560 + 2048);
        if constexpr (K == K_PL1) pol1 = pol1_ld();
        if constexpr (K == K_CH1) {
          st1 = pol1_ok() ? 1 : 0;
          cs_fin(CS_OFF + (g * 10 + 6 + w) * 2560, EX2 + wb1);
        }
        if constexpr (K == K_CH1 + 1) { if (st1 == 0) pol1 = pol1_ld(); }
        if constexpr (K == K_CH2) { if (st1 == 0 && pol1_ok()) st1 = 2; }
        if constexpr (K >= K_CH1 && K < K_CH1 + 8) { if (st1 == 1) kv_piece(K - K_CH1); }
        if constexpr (K >= K_CH2 && K < K_CH2 + 7) { if (st1 == 2) { kv_piece(K - K_CH2); if constexpr (K == K_CH2 + 6) kv_piece(7); } }
      } else {
        if constexpr (K >= K_BH && K < K_BH + 20) ldB((K - K_BH) / 5, (K - K_BH) % 5);
        if constexpr (K == K_PH1) pol1 = polh_ld();
        if constexpr (K == K_AH) rdyh = __all(pol1 == tag) ? 1 : 0;
        if constexpr (K >= K_AH && K < K_AH + 8) { if (rdyh) { ldA((K - K_AH) >> 1, 2 * (K & 1)); ldA((K - K_AH) >> 1, 2 * (K & 1) + 1); } }
      }
    }
    if constexpr (K == 71) {
#pragma unroll
      for (int i = 0; i < 2; ++i) acs[i] = lds_rd128(EX2 + (2 * nlo + i) * 1024 + l16);
    }
    if constexpr (K == 72 || K == 73) acc2[K - 72] = mfma32(acs[K - 72], bf8((u32x4){nm, nm, nm, nm}), acc2[K - 72]);
    if constexpr (K == 74) {
      const unsigned o1 = w < 5 ? 0x3f803f80u : 0u;
      if (w < 6) *(LAS u32x4*)(size_t)((w < 5 ? ONES + wb1 : MISC) + l16) = (u32x4){o1, o1, o1, o1};
    }
    SB();
  };
  rd(IC(0));
  static_rng<0, 52>([&](auto KK) { step(KK, IC(2), IC(2)); });

  auto epi2 = [&]() {
    STAMP(4);
    asm volatile("s_nop 7\n\ts_nop 7" : "+v"(acc2[0]), "+v"(acc2[1]));
#pragma unroll
    for (int i = 0; i < 2; ++i) {
#pragma unroll
      for (int z = 0; z < 4; ++z) gu2(acc2[i], z);
      gu_st(3 * p + 1 + odd, 2 * nlo + i);
    }
    SB();
  };
#define TAIL_COORDS \
  REFRESH(); \
  const int bb = opq(b), ct = (bb >> 3) & 7, rg = 4 * (bb & 7) + (bb >> 6); \
  const unsigned f2w = F2_OFF + (2 * mt + (rgp >> 1)) * 256 + (g * 4 + (rgp & 1) * 2 + nlo) * 4

  if (nlo == 0) {
    static_rng<52, 80>([&](auto KK) { step(KK, IC(2), IC(0)); });
    epi2();
    if (st1 == 0) {
#pragma unroll 1
      for (unsigned sp = 0; sp < SPIN_CAP; ++sp) {
        pol1 = pol1_ld();
        if (pol1_ok()) break;
        __builtin_amdgcn_s_sleep(4);
      }
      static_for<8>([&](auto I) { kv_piece(I); });
      wait_vm<0>();
    } else wait_vm<2>();
    wait_l0();
    asm volatile("" :: "v"(pfd));
    barrier();
    STAMP(5);
    __builtin_amdgcn_s_setprio(3);
    TAIL_COORDS;
    const int e = (bb >> 3) & 1, QB = bb >> 5, pair = 2 * (bb & 7) + ((bb >> 4) & 1);
    const int jmin = QB ? 0 : 8 - 2 * rgp;
    const unsigned ka1 = KO + (e + h) * US + (64 * rgp + n) * 16;
    const unsigned ka2 = KO + (e ? 8 : 2 * US) + (64 * rgp + n) * 16;
    unsigned va;
    {
      const int g1 = (lane >> 4) & 1, q = (lane >> 2) & 3, pp = lane & 3;
      const unsigned um = (e + (pp & 1)) * US + (pp >> 1) * 8, ut = e ? 8 : 2 * US;
      unsigned uo = VO + (g1 ? ut : um) + rgp * 1024;
      asm volatile("" : "+v"(uo));
      va = ((g1 * pp) ? ONES : uo) + (4 * h + q) * 16;
    }
    auto rdK1 = [&](int j) { return lds_rd128(ka1 + j * 512); };
    auto rdK2 = [&](int j) { return *(LAS u32x2*)(size_t)(ka2 + j * 512); };
    auto rdV = [&](int j, int sh) { return lds_trfrag(va + j * 512 + sh * 256, 128); };
    bf16x8 k1[2], vf[2][2]; u32x2 k2[2];
    k1[1] = rdK1(9); k2[1] = rdK2(9); k1[0] = rdK1(8); k2[0] = rdK2(8);
    vf[1][0] = rdV(9, 0); vf[1][1] = rdV(9, 1);
    f32x16 cc[2], o[2], sc[2][2];
#pragma unroll
    for (int a = 1; a >= 0; --a) {
      const u32x4 ku = __builtin_bit_cast(u32x4, k1[a]);
      float t = 0.f;
#pragma unroll
      for (int i = 0; i < 6; ++i)
        t = __builtin_amdgcn_fdot2_f32_bf16(__builtin_bit_cast(bf16x2, i < 4 ? ku[i] : k2[a][i - 4]),
                                            __builtin_bit_cast(bf16x2, i < 4 ? q1[a][i] : q2[a][i - 4]), t, false);
      auto r = __builtin_amdgcn_permlane32_swap(__float_as_uint(t), __float_as_uint(t), false, false);
      cc[a] = (f32x16)(-(__uint_as_float(r[0]) + __uint_as_float(r[1])));
      o[a] = (f32x16)(0.f);
    }
    sc[1][1] = mfma8(k2[1], q2[1], mfma32(k1[1], bf8(q1[1]), cc[1]));
    unsigned mk[8];
#pragma unroll
    for (int r = 0; r < 8; ++r) {
      const int c = n - 4 * h - ((2 * r & 3) + 8 * (r >> 1));
      mk[r] = (c >= 0 ? 0xffffu : 0u) | (c >= 1 ? 0xffff0000u : 0u);
    }
    SB();
    auto expk = [&](const f32x16& s, int sh, int mode) {
      u32x4 pu;
#pragma unroll
      for (int r = 0; r < 4; ++r) {
        pu[r] = pk2(__builtin_amdgcn_exp2f(s[8 * sh + 2 * r]), __builtin_amdgcn_exp2f(s[8 * sh + 2 * r + 1]));
        if (mode == 1) pu[r] &= mk[4 * sh + r];
        if (mode == 2) pu[r] &= ~mk[4 * sh + r];
      }
      return bf8(pu);
    };
    static_for<10>([&](auto TT) {
      constexpr int J = 9 - TT, cu = J & 1, nx = cu ^ 1;
      constexpr bool hasA = J <= 8, hasB = J >= 1, nA = J >= 1, nB = J >= 2;
      constexpr int mA = J == 8 ? 1 : J == 0 ? 2 : 0, mB = J == 9 ? 1 : J == 1 ? 2 : 0;
      if (J >= jmin) {
        bf16x8 pp;
        if constexpr (nB) sc[nx][1] = mfma32(k1[nx], bf8(q1[1]), cc[1]);
        if constexpr (hasA) pp = expk(sc[cu][0], 0, mA);
        if constexpr (nA) sc[nx][0] = mfma32(k1[nx], bf8(q1[0]), cc[0]);
        if constexpr (J >= 2) k1[cu] = rdK1(J - 2);
        if constexpr (J >= 1) vf[nx][0] = rdV(J - 1, 0);
        SB();
        if constexpr (hasA) { o[0] = mfma32(vf[cu][0], pp, o[0]); pp = expk(sc[cu][0], 1, mA); }
        if constexpr (nB) sc[nx][1] = mfma8(k2[nx], q2[1], sc[nx][1]);
        if constexpr (J >= 1) vf[nx][1] = rdV(J - 1, 1);
        SB();
        if constexpr (hasA) o[0] = mfma32(vf[cu][1], pp, o[0]);
        if constexpr (hasB) pp = expk(sc[cu][1], 0, mB);
        if constexpr (nA) sc[nx][0] = mfma8(k2[nx], q2[0], sc[nx][0]);
        if constexpr (J >= 2) k2[cu] = rdK2(J - 2);
        SB();
        if constexpr (hasB) {
          o[1] = mfma32(vf[cu][0], pp, o[1]); pp = expk(sc[cu][1], 1, mB);
          o[1] = mfma32(vf[cu][1], pp, o[1]);
        }
        SB();
      }
      if constexpr (J == 8) {
        wait_vm<0>();
        st32(rws, lane == 0 ? 0u : OOB, f2w, tag);
        SB();
      }
    });
    STAMP(6);
#pragma unroll
    for (int a = 0; a < 2; ++a) {
      float amax = 0.f;
#pragma unroll
      for (int r = 0; r < 12; ++r) amax = __builtin_fmaxf(amax, __builtin_fabsf(o[a][r]));
      const bool okv = (o[a][12] >= 9.0e-13f) & (o[a][12] <= 1.0e12f) & (amax <= 3.0e38f);
      if (!__all(okv)) {
        float mx = -__builtin_inff();
#pragma unroll 1
        for (int pass = 0; pass < 2; ++pass) {
          o[a] = (f32x16)(0.f);
#pragma unroll 1
          for (int j = (jmin > a ? jmin : a); j < 9 + a; ++j) {
            f32x16 s = mfma8(rdK2(j), q2[a], mfma32(rdK1(j), bf8(q1[a]), (f32x16)(0.f)));
            const unsigned fl = j == a ? ~0u : 0u, al = (j == a || j == 8 + a) ? 0u : ~0u;
#pragma unroll
            for (int r = 0; r < 8; ++r) {
              const int m = (int)((mk[r] ^ fl) | al);
              s[2 * r] = (m & 1) ? s[2 * r] : -__builtin_inff();
              s[2 * r + 1] = m < 0 ? s[2 * r + 1] : -__builtin_inff();
            }
            if (pass == 0) {
#pragma unroll
              for (int r = 0; r < 16; ++r) mx = __builtin_fmaxf(mx, s[r]);
            } else {
#pragma unroll
              for (int r = 0; r < 16; ++r) s[r] -= mx;
              o[a] = mfma32(rdV(j, 0), expk(s, 0, 0), o[a]);
              o[a] = mfma32(rdV(j, 1), expk(s, 1, 0), o[a]);
            }
          }
          if (pass == 0) {
            auto t2 = __builtin_amdgcn_permlane32_swap(__float_as_uint(mx), __float_as_uint(mx), false, false);
            mx = __builtin_fmaxf(__uint_as_float(t2[0]), __uint_as_float(t2[1]));
          }
        }
      }
      const float il = __builtin_amdgcn_rcpf(o[a][12]);
      const unsigned so = A2O_OFF + (16 * QB + 4 * rgp + 2 * a) * 20480 + 5 * pair * 256;
      const unsigned vb = (n >> 4) * 20480 + (n & 15) * 16;
      st128(rws, vb + h * 256, so + 3 * e * 256,
            (u32x4){pk2(o[a][0] * il, o[a][1] * il), pk2(o[a][2] * il, o[a][3] * il), pk2(o[a][4] * il, o[a][5] * il), pk2(o[a][6] * il, o[a][7] * il)});
      st64(rws, h ? OOB : vb, so + 512 + 8 * e, (u32x2){pk2(o[a][8] * il, o[a][9] * il), pk2(o[a][10] * il, o[a][11] * il)});
    }
    SB();
    lds_inc(CNTA);
    const u32x4 blast = ld128(rws, l16, B2O_OFF + ct * 102400 + 99 * 1024);
    wait_vm<1>();
    st32(rws, lane == 0 ? 0u : OOB, F3_OFF + (4 * QB + rgp) * 128 + (2 * pair + e) * 4, tag);
    TSTAMP(7);
    unsigned p3 = ld32(rws, n * 4, F3_OFF + rg * 128);
    lds_wait(CNTD, 4);
    TSTAMP(8);
    bf16x8 bo[4][25];
    unsigned ba[4];
#pragma unroll
    for (int P = 0; P < 4; ++P) ba[P] = BO + ((P + ct) & 3) * 25600 + l16;
    auto rdB = [&](int P, int i) {
      bo[P][i] = lds_rd128(ba[P] + i * 1024);
      if (i == 24) bo[P][i] = ((P + ct) & 3) == 3 ? bf8(blast) : bo[P][i];
    };
    static_for<25>([&](auto I) { rdB(0, I); });
    SB();
    if (!__all(p3 == tag)) {
#pragma unroll 1
      for (unsigned sp = 0; sp < SPIN_CAP; ++sp) {
        p3 = ld32(rws, n * 4, F3_OFF + rg * 128);
        if (__all(p3 == tag)) break;
        __builtin_amdgcn_s_sleep(2);
      }
    }
    SB();
    TSTAMP(9);
    u32x4 ao[4][5];
#pragma unroll
    for (int P = 0; P < 4; ++P) {
      const unsigned so = A2O_OFF + (4 * rg + rgp) * 20480 + ((P + ct) & 3) * 5120;
#pragma unroll
      for (int i = 0; i < 5; ++i) ao[P][i] = i < 4 ? ld128(rws, l16 + i * 1024, so) : ld128(rws, l16, so + 4096);
    }
    SB();
    f32x4 acc[5];
    static_for<20>([&](auto II) {
      constexpr int P = II / 5, i = II % 5;
      if constexpr (P < 3) static_for<5>([&](auto C) { rdB(P + 1, 5 * i + C); });
#pragma unroll
      for (int c = 0; c < 5; ++c) acc[c] = mfma16(bo[P][5 * i + c], bf8(ao[P][i]), II == 0 ? (f32x4)(0.f) : acc[c]);
      SB();
    });
    TSTAMP(10);
    const unsigned oo = (64 * rg + 16 * rgp + (lane & 15)) * 1280 + ct * 160 + (lane >> 4) * 8;
    lds_wait(RDYB + rgp * 4, 1);
#pragma unroll
    for (int c = 0; c < 5; ++c) {
      const f32x4 t = acc[c] + lds_rdf4(rgp * 15360 + c * 1024 + l16);
      *(u32x2*)(out + oo + c * 32) = (u32x2){pk2(t[0], t[1]), pk2(t[2], t[3])};
    }
    STAMP(15);
    STAMP_FLUSH(0);
    asm volatile("fused_lx:");
    __builtin_amdgcn_endpgm();
  } else {
    static_rng<52, 80>([&](auto KK) { step(KK, IC(2), IC(1)); });
    epi2();
    wait_l0();
    barrier();
    STAMP(5);
    TAIL_COORDS;
    if (!rdyh) {
#pragma unroll 1
      for (unsigned sp = 0; sp < SPIN_CAP; ++sp) {
        pol1 = polh_ld();
        if (__all(pol1 == tag)) break;
        __builtin_amdgcn_s_sleep(4);
      }
      static_for<16>([&](auto I) { ldA(I / 4, I % 4); });
    }
    asm volatile("" :: "v"(aq[3][3]), "v"(aq[3][2]), "v"(aq[3][1]), "v"(aq[3][0]), "v"(bq[3][4]));
    SB();
    f32x4 acc[4][5];
    auto slot = [&](int s) {
#pragma unroll
      for (int r = 0; r < 4; ++r)
#pragma unroll
        for (int c = 0; c < 5; ++c) acc[r][c] = mfma16(bf8(bq[s][c]), bf8(aq[s][r]), s == 0 ? (f32x4)(0.f) : acc[r][c]);
    };
    const unsigned bsrc = B2O_OFF + ct * 102400 + rgp * 25600, bdst = BO + rgp * 25600;
    auto dmaB = [&](int i) {
      if (i < 6) dma_lin<4>(l16, sdw, bsrc + i * 4096, bdst + i * 4096);
      else dma_lin<1>(l16, sdw, bsrc + (rgp < 3 ? 24576 : 23552), bdst + (rgp < 3 ? 24576 : 23552));
    };
    dmaB(0); dmaB(1); SB(); slot(0); SB();
    static_for<5>([&](auto C) { ldB(4, C); });
    dmaB(2); dmaB(3); SB(); slot(1); SB();
    wait_vm<21>();
    asm volatile("" :: "v"(pfd));
    st32(rws, lane == 0 ? 0u : OOB, f2w, tag);
    static_for<5>([&](auto C) { ldB(5, C); });
    dmaB(4); dmaB(5); dmaB(6); SB(); slot(2); SB();
    static_for<10>([&](auto C) { ldB(6 + C / 5, C % 5); });
    SB(); slot(3); SB();
    STAMP(6);
    unsigned p2 = ld32(rws, (lane & 15) * 4, F2_OFF + rg * 256 + rgp * 64);
    wait_vm<11>();
    lds_inc(CNTD);
    SB();
    if (!__all(p2 == tag)) {
#pragma unroll 1
      for (unsigned sp = 0; sp < SPIN_CAP; ++sp) {
        p2 = ld32(rws, (lane & 15) * 4, F2_OFF + rg * 256 + rgp * 64);
        if (__all(p2 == tag)) break;
        __builtin_amdgcn_s_sleep(2);
      }
    }
    SB();
    TSTAMP(7);
    static_for<12>([&](auto I) { ldA(4 + I / 4, I % 4); });
    SB();
    u32x2 xres[5];
    static_rng<4, 12>([&](auto SS) {
      constexpr int s = SS;
      slot(s); SB();
      if constexpr (s + 3 < 12) static_for<4>([&](auto I) { ldA(s + 3, I); });
      if constexpr (s + 4 < 12) static_for<5>([&](auto C) { ldB(s + 4, C); });
      if constexpr (s == 9) {
        const unsigned oo = (64 * rg + 16 * rgp + (lane & 15)) * 1280 + ct * 160 + (lane >> 4) * 8;
#pragma unroll
        for (int c = 0; c < 5; ++c) xres[c] = *(const u32x2*)(x + oo + c * 32);
      }
      SB();
    });
    TSTAMP(8);
    lds_wait(CNTA, 4);
#pragma unroll
    for (int d = 1; d < 4; ++d) {
      const unsigned a = ((rgp + d) & 3) * 15360 + (d - 1) * 5120 + l16;
#pragma unroll
      for (int c = 0; c < 5; ++c) *(LAS f32x4*)(size_t)(a + c * 1024) = acc[d][c];
    }
    lds_inc(CNTY);
    lds_wait(CNTY, 4);
    TSTAMP(9);
    const unsigned a = rgp * 15360 + l16;
    f32x4 t[5];
#pragma unroll
    for (int c = 0; c < 5; ++c) {
      t[c] = (acc[0][c] + lds_rdf4(a + c * 1024)) + (lds_rdf4(a + 5120 + c * 1024) + lds_rdf4(a + 10240 + c * 1024));
      t[c] += (f32x4){lo_f(xres[c][0]), hi_f(xres[c][0]), lo_f(xres[c][1]), hi_f(xres[c][1])};
    }
    SB();
#pragma unroll
    for (int c = 0; c < 5; ++c) *(LAS f32x4*)(size_t)(a + c * 1024) = t[c];
    asm volatile("" ::: "memory");
    *(volatile LAS unsigned*)(size_t)(RDYB + rgp * 4) = 1u;
    TSTAMP(10);
  }
  STAMP(15);
  STAMP_FLUSH(0);
  asm volatile("fused_ly:");
}

extern "C" void kernel_launch(void* const* d_in, const int* in_sizes, int n_in, void* d_out, int out_size,
                              void* d_ws, size_t ws_size, hipStream_t stream) {
  (void)hipFuncSetAttribute((const void*)fused, hipFuncAttributeMaxDynamicSharedMemorySize, 163840);
  hipLaunchKernelGGL(fused, dim3(256), dim3(512), 163840, stream, (const char*)d_in[0], (const char*)d_in[1],
                     (const char*)d_in[2], (const char*)d_in[4], (const char*)d_in[5], (const char*)d_in[6],
                     (char*)d_ws, (char*)d_out);
}
```

```cpp
#include <hip/hip_runtime.h>
#include <hip/hip_bf16.h>
#include <stdint.h>
#include <utility>

typedef __attribute__((ext_vector_type(2))) __bf16 bf16x2;
typedef __attribute__((ext_vector_type(4))) __bf16 bf16x4;
typedef __attribute__((ext_vector_type(8))) __bf16 bf16x8;
typedef __attribute__((ext_vector_type(4))) short s16x4;
typedef __attribute__((ext_vector_type(2))) float f32x2;
typedef __attribute__((ext_vector_type(4))) float f32x4;
typedef __attribute__((ext_vector_type(16))) float f32x16;
typedef __attribute__((ext_vector_type(2))) unsigned u32x2;
typedef __attribute__((ext_vector_type(4))) unsigned u32x4;
#define LAS __attribute__((address_space(3)))
#define DI __device__ __forceinline__
#define SB() __builtin_amdgcn_sched_barrier(0)
#define IC(v) std::integral_constant<int, (v)>{}

constexpr unsigned QKV_OFF = 0;
constexpr unsigned A2H_OFF = 8388608;
constexpr unsigned A2O_OFF = A2H_OFF + 6291456;
constexpr unsigned B2H_OFF = A2O_OFF + 2621440;
constexpr unsigned B2O_OFF = B2H_OFF + 1966080;
constexpr unsigned CS_OFF = B2O_OFF + 819200;
constexpr unsigned F0_OFF = CS_OFF + 409600;
constexpr unsigned F1_OFF = F0_OFF + 1024;
constexpr unsigned F2_OFF = F1_OFF + 8192;
constexpr unsigned FH_OFF = F2_OFF + 8192;
constexpr unsigned F3_OFF = FH_OFF + 2048;
constexpr unsigned GEN_OFF = F3_OFF + 4096;
constexpr unsigned GR_OFF = GEN_OFF + 1024;
constexpr unsigned WS_END = GR_OFF + 16384;
constexpr unsigned OOB = 0x80000000u;

#define STAMP_DECL
#define STAMP(S)
#define STAMP_FLUSH(K)

#define PSTAMP(S)
#define TSTAMP(S) STAMP(S)
constexpr float LOG2E = 1.4426950408889634f;
constexpr float QSCALE = 0.22360679774997896f * LOG2E;

template <int B, class F, int... I> DI void static_for_impl(F&& f, std::integer_sequence<int, I...>) { (f(std::integral_constant<int, B + I>{}), ...); }
template <int B, int E, class F> DI void static_rng(F&& f) { static_for_impl<B>(f, std::make_integer_sequence<int, E - B>{}); }
template <int N, class F> DI void static_for(F&& f) { static_rng<0, N>(f); }

template <int N> DI void wait_vm() { asm volatile("s_waitcnt vmcnt(%0)" :: "i"(N) : "memory"); }
template <int N, int L> DI void wait_vm_l() { asm volatile("s_waitcnt vmcnt(%0) lgkmcnt(%1)" :: "i"(N), "i"(L) : "memory"); }
template <int N> DI void wait_vm_l0() { asm volatile("s_waitcnt vmcnt(%0) lgkmcnt(0)" :: "i"(N) : "memory"); }
DI void wait_l0() { asm volatile("s_waitcnt lgkmcnt(0)" ::: "memory"); }
DI void barrier() { __builtin_amdgcn_s_barrier(); asm volatile("" ::: "memory"); }

template <unsigned LB> DI void dma_k(unsigned voff, u32x4 srd, unsigned k, unsigned step, unsigned wb) {
  unsigned t;
  asm volatile("s_add_u32 m0, %5, %6\n\ts_mul_i32 %0, %3, %4\n\tbuffer_load_dwordx4 %1, %2, %0 offen lds"
               : "=&s"(t) : "v"(voff), "s"(srd), "s"(k), "s"(step), "s"(wb), "i"(LB) : "memory", "scc");
}
template <unsigned LB> DI void dma_x(unsigned v0, unsigned v1, u32x4 srd, unsigned k, unsigned wb) {
  unsigned t;
  asm volatile("s_add_u32 m0, %5, %6\n\ts_lshl_b32 %0, %4, 7\n\tbuffer_load_dwordx4 %1, %3, %0 offen lds\n\t"
               "buffer_load_dwordx4 %2, %3, %0 offen offset:1024 lds"
               : "=&s"(t) : "v"(v0), "v"(v1), "s"(srd), "s"(k), "s"(wb), "i"(LB) : "memory", "scc");
}
#define DSC "sc1"
#define BAUX 16
template <int N> DI void dma_lin(unsigned voff, u32x4 srd, unsigned soff, unsigned lds) {
  static_assert(N >= 1 && N <= 4, "");
  if constexpr (N == 1)
    asm volatile("s_mov_b32 m0, %3\n\ts_nop 0\n\tbuffer_load_dwordx4 %0, %1, %2 offen " DSC " lds" :: "v"(voff), "s"(srd), "s"(soff), "s"(lds) : "memory");
  if constexpr (N == 2)
    asm volatile("s_mov_b32 m0, %3\n\ts_nop 0\n\tbuffer_load_dwordx4 %0, %1, %2 offen " DSC " lds\n\t"
                 "buffer_load_dwordx4 %0, %1, %2 offen offset:1024 " DSC " lds" :: "v"(voff), "s"(srd), "s"(soff), "s"(lds) : "memory");
  if constexpr (N == 3)
    asm volatile("s_mov_b32 m0, %3\n\ts_nop 0\n\tbuffer_load_dwordx4 %0, %1, %2 offen " DSC " lds\n\t"
                 "buffer_load_dwordx4 %0, %1, %2 offen offset:1024 " DSC " lds\n\t"
                 "buffer_load_dwordx4 %0, %1, %2 offen offset:2048 " DSC " lds" :: "v"(voff), "s"(srd), "s"(soff), "s"(lds) : "memory");
  if constexpr (N == 4)
    asm volatile("s_mov_b32 m0, %3\n\ts_nop 0\n\tbuffer_load_dwordx4 %0, %1, %2 offen " DSC " lds\n\t"
                 "buffer_load_dwordx4 %0, %1, %2 offen offset:1024 " DSC " lds\n\t"
                 "buffer_load_dwordx4 %0, %1, %2 offen offset:2048 " DSC " lds\n\t"
                 "buffer_load_dwordx4 %0, %1, %2 offen offset:3072 " DSC " lds" :: "v"(voff), "s"(srd), "s"(soff), "s"(lds) : "memory");
}
DI void dma_g(unsigned voff, const void* sbase, unsigned lds) {
  asm volatile("s_mov_b32 m0, %2\n\ts_nop 0\n\tglobal_load_lds_dwordx4 %0, %1" :: "v"(voff), "s"(sbase), "s"(lds) : "memory");
}

DI void warm(unsigned& d, unsigned voff, const void* sbase, unsigned long long mask) {
  asm volatile("s_mov_b64 exec, %3\n\tglobal_load_dword %0, %1, %2\n\ts_mov_b64 exec, -1" : "+v"(d) : "v"(voff), "s"(sbase), "s"(mask) : "memory");
}
DI void warm1(unsigned& d, unsigned voff, u32x4 srd, unsigned soff) {
  asm volatile("buffer_load_dword %0, %1, %2, %3 offen sc1" : "+v"(d) : "v"(voff), "s"(srd), "s"(soff) : "memory");
}
DI bf16x8 lds_rd128(unsigned a) { return *(LAS bf16x8*)(size_t)a; }
DI f32x4 lds_rdf4(unsigned a) { return *(LAS f32x4*)(size_t)a; }
DI bf16x4 lds_tr(unsigned a) { return __builtin_amdgcn_ds_read_tr16_b64_v4bf16((LAS bf16x4*)(size_t)a); }
DI bf16x8 cat8(bf16x4 lo, bf16x4 hi) { return __builtin_shufflevector(lo, hi, 0, 1, 2, 3, 4, 5, 6, 7); }
DI bf16x8 lds_trfrag(unsigned a, unsigned step) { return cat8(lds_tr(a), lds_tr(a + step)); }
DI unsigned pk2(float a, float b) {
  f32x2 v = {a, b};
  return __builtin_bit_cast(unsigned, __builtin_convertvector(v, bf16x2));
}
DI float bfr(float a) { return (float)(__bf16)a; }
DI float lo_f(unsigned u) { return __uint_as_float(u << 16); }
DI float hi_f(unsigned u) { return __uint_as_float(u & 0xffff0000u); }
DI bf16x8 bf8(u32x4 v) { return __builtin_bit_cast(bf16x8, v); }
DI f32x16 mfma32(bf16x8 a, bf16x8 b, f32x16 c) { return __builtin_amdgcn_mfma_f32_32x32x16_bf16(a, b, c, 0, 0, 0); }
DI f32x4 mfma16(bf16x8 a, bf16x8 b, f32x4 c) { return __builtin_amdgcn_mfma_f32_16x16x32_bf16(a, b, c, 0, 0, 0); }
DI f32x16 mfma8(u32x2 a, u32x2 b, f32x16 c) {
  return __builtin_amdgcn_mfma_f32_32x32x8bf16_1k(__builtin_bit_cast(s16x4, a), __builtin_bit_cast(s16x4, b), c, 0, 0, 0);
}
typedef __amdgpu_buffer_rsrc_t rsrc_t;
DI rsrc_t mkrsrc(const void* p, unsigned n) { return __builtin_amdgcn_make_buffer_rsrc((void*)p, 0, n, 0x00020000); }
DI void st128(rsrc_t r, unsigned vo, unsigned so, u32x4 v) {
  __builtin_amdgcn_raw_buffer_store_b128(v, r, vo, so, 16);
  asm volatile("s_nop 1" :: "v"(v) : "memory");
}
DI void st64(rsrc_t r, unsigned vo, unsigned so, u32x2 v) { __builtin_amdgcn_raw_buffer_store_b64(v, r, vo, so, 16); }
DI void st32(rsrc_t r, unsigned vo, unsigned so, unsigned v) { __builtin_amdgcn_raw_buffer_store_b32(v, r, vo, so, 16); }
DI u32x4 ld128(rsrc_t r, unsigned vo, unsigned so) { return __builtin_amdgcn_raw_buffer_load_b128(r, vo, so, 16); }
DI u32x4 ld128b(rsrc_t r, unsigned vo, unsigned so) { return __builtin_amdgcn_raw_buffer_load_b128(r, vo, so, BAUX); }
DI u32x2 ld64(rsrc_t r, unsigned vo, unsigned so) { return __builtin_amdgcn_raw_buffer_load_b64(r, vo, so, 16); }
DI unsigned ld32(rsrc_t r, unsigned vo, unsigned so) { return __builtin_amdgcn_raw_buffer_load_b32(r, vo, so, 16); }
DI u32x4 mkdesc(const void* p, unsigned n) {
  const unsigned long long a = (unsigned long long)p;
  return (u32x4){(unsigned)a, (unsigned)(a >> 32) & 0xffffu, n, 0x00020000u};
}
template <int CTRL, int RM> DI float dpp_add(float v) {
  return v + __uint_as_float(__builtin_amdgcn_update_dpp(0, __float_as_uint(v), CTRL, RM, 0xf, true));
}
DI int fresh_lane() {
  int l; asm volatile("v_mbcnt_lo_u32_b32 %0, -1, 0\n\tv_mbcnt_hi_u32_b32 %0, -1, %0" : "=v"(l)); return l;
}
DI int opq(int v) { asm volatile("" : "+s"(v)); return v; }
DI unsigned opq(unsigned v) { asm volatile("" : "+s"(v)); return v; }
DI unsigned vopq(unsigned v) { asm("" : "+v"(v)); return v; }
DI float vmul(float a, float b) { float r; asm("v_mul_f32 %0, %1, %2" : "=v"(r) : "v"(a), "v"(b)); return r; }
DI unsigned vadd(unsigned a, unsigned c) { unsigned r; asm volatile("v_add_u32 %0, %2, %1" : "=v"(r) : "v"(a), "s"(c)); return r; }
DI void lds_inc(unsigned a) {
  asm volatile("s_mov_b64 exec, 1\n\tds_add_u32 %0, %1\n\ts_mov_b64 exec, -1" :: "v"(a), "v"(1u) : "memory");
}
constexpr unsigned SPIN_CAP = 1u << 16;
DI void lds_wait(unsigned a, unsigned v) {
#pragma unroll 1
  for (unsigned sp = 0; sp < 64 * SPIN_CAP; ++sp) {
    if (__builtin_amdgcn_readfirstlane(*(volatile LAS unsigned*)(size_t)a) >= v) break;
    __builtin_amdgcn_s_sleep(1);
  }
  asm volatile("" ::: "memory");
}

constexpr unsigned XOFF = 98304, RTO = 49152 + 8192, EX1 = 49152, EX2 = 51200;
constexpr unsigned xsl(int s) { return XOFF + (unsigned)(s & 3) * 16384u; }
constexpr unsigned wsl1(int s) { return (unsigned)(s & 3) * 24576u; }
constexpr unsigned wsl2(int u) {
  return u == 0 ? 57344u : u == 1 ? 73728u : u == 2 ? 0u : u == 3 ? 16384u : u == 4 ? 32768u :
         u == 5 ? 131072u : u == 6 ? 57344u : u == 7 ? 147456u : u == 8 ? 73728u : 98304u;
}
constexpr unsigned US = 8256, KO = 0, VO = 3 * US;
constexpr unsigned MISC = 61440, CNTA = MISC, CNTY = MISC + 4, CNTD = MISC + 8, RDYB = MISC + 16, RDY3 = MISC + 12, CNTL = MISC + 32;
#define PSL 4
#define WAITY 6
#define ACKW
#define SERA
constexpr unsigned ONES = 55424, BO = MISC + 1024;
static_assert(6 * US <= EX2 && BO + 99 * 1024 == 163840 && (ONES & 255) == ((VO + 192) & 255) && ONES >= EX2 + 4096 && ONES + 5120 <= MISC, "lds");

constexpr int K_SL = 1, K_SM = 9, K_CP1 = 14, K_ST0 = 18, K_FL0 = 27, K_GR = 26, K_CS1 = 26, K_CF1 = 33, K_PL0 = 42, K_CK0 = 50, K_FL1 = 51, K_FH1 = 59;
#define KCH1 65
#define KCF2 67
#define KPL1 58
#define KPH1 67
#define KAH 73
constexpr int K_CP2 = 49, K_PL1 = KPL1, K_CS2 = 56, K_CH1 = KCH1, K_CF2 = KCF2, K_CH2 = 74, K_BH = 58, K_PH1 = KPH1, K_AH = KAH, K_ROLE = 52;
struct Ops { short id[320]; short k[320]; short dma[320]; int n; };
constexpr int p2_at(int K) {
  return K == 31 ? 0 : K == 32 ? 1 : K == 35 ? 2 : K == 36 ? 3 : K == 39 ? 4 : K == 40 ? 5 : K == 42 ? 6 : K == 43 ? 7 :
         K == 44 ? 8 : K == 46 ? 9 : K == 47 ? 10 : K == 48 ? 11 : (K >= 51 && K <= 58) ? K - 39 : -1;
}
constexpr Ops make_ops(bool odd, bool y) {
  Ops o{}; int n = 0;
  auto add = [&](int id, int k, int d) { o.id[n] = (short)id; o.k[n] = (short)k; o.dma[n] = (short)d; ++n; };
  for (int i = 0; i < 5; ++i) add(100 + i, -3, 1);
  add(4, -3, 0); add(2, -3, 0);
  for (int i = 5; i < 10; ++i) add(100 + i, -3, 1);
  add(5, -3, 0);
  for (int K = 0; K < 64; ++K) {
    if (K >= 3 && K <= 30) {
      const int s = ((K - 3) >> 2) + 3, rel = (K - 3) & 3;
      if (rel == 0) { add(100 + 5 * s, K, 1); add(101 + 5 * s, K, 1); } else add(101 + 5 * s + rel, K, 1);
    }
    if (p2_at(K) >= 0) add(200 + p2_at(K), K, 1);
    if (K == 0) { add(110, K, 0); add(111, K, 0); }
    if (K == 1) add(112, K, 0);
    if (K == 2) { add(113, K, 0); add(114, K, 0); }
    if (K == K_SL) for (int i = 0; i < 4; ++i) add(3, K, 0);
    if (K == K_CP1 || K == K_CP2) add(332, K, 0);
    if (K == K_ST0) { add(310, K, 0); add(310, K, 0); }
    if (K == K_GR) add(320, K, 0);
    if (K == K_CS1) { add(325, K, 2); add(325, K, 2); add(325, K, 2); }
    if (K == K_PL0) add(340, K, 2);
    if (K >= 40 && K < (odd ? 46 : 42)) add(330, K, 0);
    if (!odd && (K == 45 || K == 49)) add(331, K, 0);
    if (y && K == K_CS2) { add(350, K, 0); add(350, K, 0); }
    if (y && K == K_CS2 + 1) add(350, K, 0);
    if (!y && K == K_PL1) add(350, K, 0);
    if (y && K >= K_BH) add(350, K, 0);
  }
  o.n = n; return o;
}
constexpr Ops OPS[4] = {make_ops(false, false), make_ops(true, false), make_ops(false, true), make_ops(true, true)};
constexpr bool stage_has(int id, int S) {
  return (id >= 100 && id < 200) ? (id - 100) / 5 <= S : (id >= 200 && id < 300) ? (id - 200) / 2 + 10 <= S : false;
}
constexpr int cmin(int a, int b) { return a < b ? a : b; }
constexpr int clampw(int w) { return w < 0 ? 0 : (w > 63 ? 63 : w); }
constexpr int wait_bar(const Ops& o, int K) {
  const int S = K == 63 ? 17 : (K >> 2) + 1;
  int last = -1, c = 0;
  for (int i = 0; i < o.n; ++i)
    if (stage_has(o.id[i], S) || (K == K_FL0 && o.id[i] == 310) || (K == K_FL1 && o.id[i] == 330) || (K == K_FH1 && o.id[i] == 331)) last = i;
  for (int i = last + 1; i < o.n; ++i) if (o.k[i] < K && o.dma[i] != 2) ++c;
  return clampw(c);
}
constexpr int wait_mid(const Ops& o, int K, int id) {
  int last = -1, c = 0;
  for (int i = 0; i < o.n; ++i) if (o.id[i] == id) last = i;
  for (int i = last + 1; i < o.n; ++i) if ((o.k[i] < K && o.dma[i] != 2) || (o.k[i] == K && o.dma[i] == 1)) ++c;
  return clampw(c);
}

__global__ __launch_bounds__(512, 2) void fused(const char* __restrict__ x, const char* __restrict__ wqkv,
                                                const char* __restrict__ wo, const char* __restrict__ wg,
                                                const char* __restrict__ wu, const char* __restrict__ wd,
                                                char* __restrict__ ws, char* __restrict__ out) {
  asm volatile("" :: "s"(x), "s"(wqkv), "s"(wo), "s"(wg), "s"(wu), "s"(wd), "s"(ws), "s"(out));
  const int b = blockIdx.x;
  const int mt = 8 * (b & 1) + (b >> 5), g = 4 * ((b >> 1) & 3) + ((b >> 3) & 3), odd = g & 1, p = g >> 1;
  const int w = __builtin_amdgcn_readfirstlane(threadIdx.x >> 6);
  const int rgp = w & 3, nlo = w >> 2;
  int lane = threadIdx.x & 63, h = lane >> 5, n = lane & 31;
  unsigned l16 = lane * 16;
#define REFRESH() do { lane = fresh_lane(); h = lane >> 5; n = lane & 31; l16 = lane * 16; } while (0)
#define KB0 42
#define KB1 46
#define KB2 50
#define ROT 0
  const int ag = (0x8530 >> (4 * (g & 3))) & 15;
  const int own = (mt & 7) + 2 * (mt >> 3), pos = (mt < 8 || mt >= 14) ? 0 : 5;
  const int r0 = (own + ag + 2 * (g >> 2)) % 10;
  STAMP_DECL; STAMP(0);

  unsigned pfd = 0;
  const unsigned wb1 = w * 1024, wb2 = w * 2048;
  const u32x4 sdx = mkdesc(x + (size_t)(128 * mt + 16 * w) * 1280, 0x40000000u);
  const u32x4 sdq = mkdesc(wqkv + (size_t)(8 * w) * 3840, 0x40000000u);
  const u32x4 sd1 = mkdesc(odd ? wqkv + (size_t)(8 * w) * 3840 : wg + (size_t)(8 * w) * 3072, 0x40000000u);
  const u32x4 sd2 = mkdesc(odd ? wqkv + (size_t)(8 * w) * 3840 : wu + (size_t)(8 * w) * 3072, 0x40000000u);
  const unsigned step12 = odd ? 245760u : 196608u;
  unsigned xv0, xv1, wv[3];
  {
    const int r = lane >> 3, u = lane & 7, sw = ((r >> 1) & 1) << 2;
    xv0 = r * 1280 + ((u ^ (r >> 1)) << 4);
    xv1 = (r + 8) * 1280 + ((u ^ (4 + (r >> 1))) << 4) - 1024;
    const unsigned vq = r * 3840 + ((u ^ sw) << 4);
    wv[0] = vq + (4 * p + odd) * 128;
    wv[1] = odd ? vq + (p == 7 ? 29 : 4 * p + 2) * 128 : r * 3072 + ((u ^ sw) << 4) + 3 * p * 128;
    wv[2] = odd ? vq + (p == 7 ? 29 : 4 * p + 3) * 128 : r * 3072 + ((u ^ sw ^ 2) << 4) + 3 * p * 128;
  }
  auto kst = [&](int pos) { int k = r0 + pos; return (unsigned)(k >= 10 ? k - 10 : k); };
  auto issue1 = [&](auto SS, auto RR) {
    constexpr int s = SS, rel = RR;
    if constexpr (rel == 0) dma_x<xsl(s)>(xv0, xv1, sdx, kst(s), wb2);
    else dma_k<wsl1(s) + (rel - 1) * 8192>(wv[rel - 1], rel == 1 ? sdq : rel == 2 ? sd1 : sd2, kst(s), rel == 1 ? 245760u : step12, wb1);
  };
  PSTAMP(7);
  static_for<4>([&](auto I) { issue1(IC(0), I); });
  SB();
  PSTAMP(8);
  barrier();
  STAMP(1);
  {
    unsigned long long pc; unsigned len, l2;
    asm volatile("s_getpc_b64 %0\n1:\ts_mov_b32 %1, fused_lx-1b\n\ts_mov_b32 %2, fused_ly-1b\n\ts_max_u32 %1, %1, %2"
                 : "=s"(pc), "=s"(len), "=s"(l2) :: "scc");
    const u32x4 sdc = {(unsigned)pc, (unsigned)(pc >> 32) & 0xffffu, len, 0x00020000u};
    asm volatile("buffer_load_dword %0, %1, %2, %3 offen" : "+v"(pfd) : "v"(lane < 2 ? lane * 128 : OOB), "s"(sdc), "s"(((b >> 3) * 8 + w) * 256) : "memory");
  }
  u32x4 sx4; unsigned sx1, gen;
  const int srow = 128 * mt + 8 * g + w;
  asm volatile("global_load_dword %0, %1, %2 sc1" : "=v"(gen) : "v"(0), "s"(ws + GEN_OFF) : "memory");
  static_for<4>([&](auto I) { issue1(IC(1), I); });
  asm volatile("buffer_load_dword %0, %1, %2, 0 offen" : "+v"(pfd) : "v"(OOB), "s"(sdx) : "memory");
  const int jok = b < 170, jb = jok ? b : b - 170;
  const int jG = (jb * 241) >> 12, jks = (jb - 17 * jG) * 4 + (w >> 1);
  SB();
  PSTAMP(9);

  unsigned wa1[3], wa2[2], xa[4];
  {
    const int cb = (lane >> 4) & 1, q = (lane >> 2) & 3, pl = (lane >> 1) & 1, ph = lane & 1;
    const unsigned rowoff = (8 * h + q) * 128 + pl * 8;
    const int sw = (q >> 1) << 2;
#pragma unroll
    for (int i = 0; i < 3; ++i) {
      const int j = 3 * nlo + i;
      const int piece = odd ? (j >> 1) : 0, sel = odd ? (j & 1) : nlo, t = 2 * nlo + i - 1;
      const unsigned aq = piece * 8192 + (((4 * sel + 2 * cb + ph) ^ sw) << 4);
      const unsigned ag = (1 + cb) * 8192 + (((2 * t + ph) ^ sw ^ (cb << 1)) << 4);
      wa1[i] = rowoff + ((odd || i == 0) ? aq : ag);
    }
#pragma unroll
    for (int i = 0; i < 2; ++i) wa2[i] = rowoff + cb * 8192 + (((2 * (2 * nlo + i) + ph) ^ sw ^ (cb << 1)) << 4);
    const int row = 32 * rgp + n, sx = (row >> 1) & 7;
#pragma unroll
    for (int j = 0; j < 4; ++j) xa[j] = XOFF + row * 128 + (((2 * j + h) ^ sx) << 4);
  }
  auto mksel = [&]() {
    const int fl = fresh_lane(), i = fl & 15, kg = fl >> 4;
    const unsigned o1 = ((i & 3) | (((i >> 2) ^ kg) & 1)) == 0 ? 0x3f803f80u : 0u;
    return bf8((u32x4){o1, o1, o1, o1});
  };
  const rsrc_t rws = mkrsrc(ws, WS_END);
  const u32x4 sdw = mkdesc(ws, WS_END);
  SB();
  PSTAMP(10);
  wait_vm<8>();
  PSTAMP(14);
  barrier();
  STAMP(2);

  f32x16 acc1[3], acc2[2];
  f32x4 cs = (f32x4)(0.f);
  bf16x8 xr[56];
  bf16x8 wf[2][3], csf[4];
  unsigned wcs = rgp == 0 ? vopq(wa1[0]) : rgp == 1 ? vopq(wa1[1]) : vopq(wa1[2]);
  unsigned wcur[3];
  bf16x8 rt[2];
  u32x2 grn;
  u32x4 cl[3];
  unsigned tag, nm;
  float sp0, ss1, ss2;
  bf16x8 acs[3];
  float rstd, sdv, gsc;
  u32x4 pol, hq, sdg, sdu;
  unsigned pol1, st1 = 0, vs, vh, gv2, uv2;
  u32x4 q1[2]; u32x2 q2[2];
  u32x4 bq[12][5], aq[12][4];

  auto rd = [&](auto KK) {
    constexpr int K = KK;
    if constexpr (K < 40) {
      constexpr int s = K >> 2, j = K & 3;
      xr[K] = lds_rd128(xa[j] + (xsl(s) - XOFF));
      if constexpr (j == 0 && (s & 3) == 3) {
#pragma unroll
        for (int i = 0; i < 3; ++i) wcur[i] = vadd(wa1[i], wsl1(s));
      }
#pragma unroll
      for (int i = 0; i < 3; ++i) wf[K & 1][i] = lds_trfrag(((s & 3) == 3 ? wcur[i] : wa1[i] + wsl1(s)) + j * 2048, 512);
    } else if constexpr (K < 80) {
      constexpr int u = (K - 40) >> 2, j = K & 3;
      if constexpr (u < 4) xr[K] = lds_rd128(xa[j] + (xsl(6 + u) - XOFF));
      constexpr bool far = wsl2(u) > 57344;
      if constexpr (j == 0 && far) {
#pragma unroll
        for (int i = 0; i < 2; ++i) wcur[i] = vadd(wa2[i], wsl2(u));
      }
#pragma unroll
      for (int i = 0; i < 2; ++i) wf[K & 1][i] = lds_trfrag((far ? wcur[i] : wa2[i] + wsl2(u)) + j * 2048, 512);
    }
  };
  auto cs_ok = [&]() {
    return (((cl[0][1] ^ tag) | (cl[0][3] ^ tag)) | ((cl[1][1] ^ tag) | (cl[1][3] ^ tag)) | (((cl[2][1] ^ tag) | (cl[2][3] ^ tag)) & (unsigned)(h - 1))) == 0;
  };
  auto cs_fin = [&](unsigned src, unsigned dst) {
    if (!__all(cs_ok())) {
#pragma unroll 1
      for (unsigned sp = 0; sp < SPIN_CAP; ++sp) {
        __builtin_amdgcn_s_sleep(4);
        cl[0] = ld128(rws, l16, src); cl[1] = ld128(rws, l16 + 1024, src); cl[2] = ld128(rws, h ? OOB : l16, src + 2048);
        if (__all(cs_ok())) break;
      }
    }
    const float c = ((__uint_as_float(cl[0][0]) + __uint_as_float(cl[0][2])) + (__uint_as_float(cl[1][0]) + __uint_as_float(cl[1][2]))) +
                    (__uint_as_float(cl[2][0]) + __uint_as_float(cl[2][2]));
    const unsigned u0 = __float_as_uint(c);
    const float r1 = c - __uint_as_float(u0 & 0xffff0000u);
    const unsigned u1 = __float_as_uint(r1);
    const float r2 = r1 - __uint_as_float(u1 & 0xffff0000u);
    *(LAS u32x4*)(size_t)(dst + l16) = (u32x4){__builtin_amdgcn_perm(u1, u0, 0x07060302u), __float_as_uint(r2) >> 16, 0u, 0u};
  };
  auto qkv_st = [&](const f32x16& a16, int cu0, int a) {
    const float r = rstd * (cu0 < 80 ? QSCALE : 1.f);
    u32x4 val;
#pragma unroll
    for (int i = 0; i < 4; ++i) val[i] = pk2(vmul(a16[8 * a + 2 * i], r), vmul(a16[8 * a + 2 * i + 1], r));
    st128(rws, vs, QKV_OFF + (cu0 + 2 * a) * 32768 + (128 * mt + 32 * rgp) * 16, val);
  };
  auto gu2 = [&](const f32x16& a16, int i) {
    float t0, t1; unsigned r;
    asm("v_mul_f32 %1, %3, %7\n\tv_mul_f32 %2, %4, %7\n\tv_exp_f32 %1, %1\n\tv_exp_f32 %2, %2\n\t"
        "v_fma_f32 %1, %1, %8, %8\n\tv_fma_f32 %2, %2, %8, %8\n\tv_rcp_f32 %1, %1\n\tv_rcp_f32 %2, %2\n\t"
        "v_mul_f32 %1, %5, %1\n\tv_mul_f32 %2, %6, %2\n\tv_cvt_pk_bf16_f32 %0, %1, %2"
        : "=v"(r), "=&v"(t0), "=&v"(t1)
        : "v"(a16[2 * i]), "v"(a16[2 * i + 1]), "v"(a16[8 + 2 * i]), "v"(a16[9 + 2 * i]), "v"(gsc), "v"(sdv));
    hq[i] = r;
  };
  auto gu_st = [&](int unit, int t) {
    st128(rws, vh, A2H_OFF + (((2 * mt + (rgp >> 1)) * 48 + 2 * unit + (t >> 1)) * 4 + 2 * (rgp & 1)) * 1024 + (t & 1) * 512, hq);
  };
  auto kv_piece = [&](int c) {
    const int bb = opq(b), pair = 2 * (bb & 7) + ((bb >> 4) & 1), e = (bb >> 3) & 1, QB = bb >> 5;
    int t0 = 256 * QB - 256 + 128 * rgp; t0 = t0 < 0 ? 0 : t0;
    if (c < 6) dma_lin<2>(l16, sdw, QKV_OFF + (80 + 80 * (c / 3) + 5 * pair + 2 * e + c % 3) * 32768 + t0 * 16, c * US + rgp * 2048);
    else {
      const unsigned so = QKV_OFF + 5 * pair * 32768 + (256 * QB + 64 * rgp + 32 * (c - 6)) * 16;
      q1[c - 6] = ld128(rws, vs, so + 3 * e * 32768);
      q2[c - 6] = ld64(rws, h ? OOB : n * 16, so + 2 * 32768 + 8 * e);
    }
  };
  auto pol1_on = [&]() { return (lane < 24) & (lane >= (opq(b) >= 32 ? 0 : 12)); };
  auto pol1_ld = [&]() {
    const int bb = opq(b);
    return ld32(rws, pol1_on() ? lane * 4 : OOB, F1_OFF + ((bb & 7) * 8 + (bb >> 5)) * 128);
  };
  auto pol1_ok = [&]() { return __all((pol1 == tag) | !pol1_on()); };
  unsigned av[4], l16b, kt1, kt2, abase, bbase, soa, sob;
  auto ksof = [&](int s) { return ((s < 4 ? opq(kt1) >> (4 * s) : opq(kt2) >> (4 * (s - 4))) & 15u); };
  auto ldB = [&](int s, int c) {
    if (c == 0) sob = bbase + ksof(s) * 5120;
    bq[s][c] = c < 4 ? ld128b(rws, l16 + c * 1024, sob) : ld128b(rws, l16b, sob);
  };
  auto ldA = [&](int s, int r) {
    if (r == 0) soa = abase + ksof(s) * 4096;
    aq[s][r] = ld128b(rws, av[r], soa);
  };
  auto polh_ld = [&]() {
    const int bb = opq(b);
    return ld32(rws, (lane & 1) * 4, FH_OFF + (2 * (bb & 7) + (bb >> 7)) * 128 + rgp * 8);
  };

  auto step = [&](auto KK, auto OD, auto RL) {
    constexpr int K = KK, j = K & 3, ODD = OD, ROLE = RL;
    if constexpr (K == 40) STAMP(3);
    if constexpr (j == 3 && K <= 63) {
      constexpr int nx = ODD < 2 ? wait_bar(OPS[ODD & 1], K) : cmin(wait_bar(OPS[0], K), wait_bar(OPS[1], K));
      constexpr int ny = ODD < 2 ? wait_bar(OPS[2 + (ODD & 1)], K) : cmin(wait_bar(OPS[2], K), wait_bar(OPS[3], K));
      static_assert(ROLE < 2 || nx == ny, "");
      if constexpr (K == 35) wait_vm_l<nx, 7>(); else wait_vm<(ROLE == 1 ? ny : nx)>();
      barrier();
      if constexpr (K == K_FL0) { if (w == 0) st32(rws, fresh_lane() == 0 ? 0u : OOB, F0_OFF + b * 4, tag); }
      if constexpr (K == K_FL1) {
        if (w == 0) {
          const int l = fresh_lane(), ui = l >> 2, hi = (l >> 1) & 1, qi = l & 1;
          const int u = 4 * p + odd + ui;
          const int G = ((u * 4 * 205) >> 10) + hi;
          const int fu = (5 * G) >> 2;
          const int qb = (mt >> 1) + qi;
          const bool on = (ui < (odd ? 3 : 1)) & (u < 30) & (fu <= u) & (qb < 8);
          const unsigned a = ((G & 7) * 8 + qb) * 128 + ((mt - 2 * qb + 2) * 6 + (G >> 3) * 2 + (u - fu)) * 4;
          st32(rws, on ? a : OOB, F1_OFF, tag);
        }
      }
      if constexpr (K == K_FH1 && ROLE == 0) { if (w == 0 && !odd) st32(rws, fresh_lane() == 0 ? 0u : OOB, FH_OFF + mt * 128 + p * 4, tag); }
    }
    if constexpr (K == 71) {
      static_assert(K_CH1 < 71 && K_CF2 < 71 && K_PL1 >= 58 && K_PH1 > 58 && K_PH1 < 71 && K_BH == 58 && K_AH >= 71, "");
      if constexpr (ROLE == 1) wait_vm_l<71 - K_BH + 1, (K_CF2 == 70 ? 0 : 4)>();
      barrier();
    }
    rd(IC(K + 1));
    if constexpr (K >= 3 && K <= 30) issue1(IC(((K - 3) >> 2) + 3), IC((K - 3) & 3));
    if constexpr (p2_at(K) >= 0) {
      constexpr int u = p2_at(K) >> 1, jj = p2_at(K) & 1;
      dma_k<wsl2(u) + jj * 8192>(jj ? uv2 : gv2, jj ? sdu : sdg, kst((u + 6) % 10), 196608u, wb1);
    }
    if constexpr (K < 40) {
#pragma unroll
      for (int i = 0; i < 3; ++i) acc1[i] = mfma32(wf[K & 1][i], xr[K], K == 0 ? (f32x16)(0.f) : acc1[i]);
      __builtin_amdgcn_sched_group_barrier(0x8, 1, 0); __builtin_amdgcn_sched_group_barrier(0x100, 3, 0);
      __builtin_amdgcn_sched_group_barrier(0x8, 1, 0); __builtin_amdgcn_sched_group_barrier(0x100, 2, 0);
      __builtin_amdgcn_sched_group_barrier(0x8, 1, 0); __builtin_amdgcn_sched_group_barrier(0x100, 2, 0);
    } else {
      constexpr int u = (K - 40) >> 2;
#pragma unroll
      for (int i = 0; i < 2; ++i) acc2[i] = mfma32(wf[K & 1][i], xr[u < 4 ? K : K - 56], K == 40 ? (f32x16)(0.f) : acc2[i]);
      __builtin_amdgcn_sched_group_barrier(0x8, 1, 0); __builtin_amdgcn_sched_group_barrier(0x100, 3, 0);
      __builtin_amdgcn_sched_group_barrier(0x8, 1, 0); __builtin_amdgcn_sched_group_barrier(0x100, 5, 0);
    }
    SB();
    if constexpr (K < 40) asm volatile("" :: "v"(acc1[0]), "v"(acc1[1]), "v"(acc1[2]));
    else asm volatile("" :: "v"(acc2[0]), "v"(acc2[1]));
    if constexpr (K == 0 || K == 3 || K == 7 || K == 11 || K == 15) {
      if (pos == (K + 1) >> 2) {
#pragma unroll
        for (int i = 0; i < 4; ++i) csf[i] = lds_trfrag(wcs + wsl1((K + 1) >> 2) + i * 2048, 512);
      }
    }
    if constexpr (K == 47) {
      const unsigned a = vadd(wcs, pos < 2 ? wsl2(0) + pos * 16384 : (pos - 2) * 16384);
      if (pos < 5) {
#pragma unroll
        for (int i = 0; i < 4; ++i) csf[i] = lds_trfrag(a + i * 2048, 512);
      }
    }
    if constexpr (K == 1 || K == 4 || K == 8 || K == 12 || K == 16 || K == 48) {
      if (K == 48 ? pos < 5 : pos == K >> 2) {
        const bf16x8 selv = mksel();
        cs = (f32x4)(0.f);
#pragma unroll
        for (int i = 0; i < 4; ++i) cs = mfma16(selv, csf[i], cs);
      }
    }
    if constexpr (K == 0) issue1(IC(2), IC(0));
    if constexpr (K == 1) issue1(IC(2), IC(1));
    if constexpr (K == 2) { issue1(IC(2), IC(2)); issue1(IC(2), IC(3)); }
    if constexpr (K == K_SL) {
      const char* rp = x + (size_t)srow * 1280;
      asm volatile("global_load_dwordx4 %0, %1, %2" : "=v"(sx4) : "v"(lane * 16), "s"(rp) : "memory");
      asm volatile("global_load_dword %0, %1, %2 offset:1024" : "=v"(sx1) : "v"(lane * 4), "s"(rp) : "memory");
      const int ko = jks - 48, oP = (ko * 205) >> 10, oi = ko - 5 * oP;
      const bool tl = jks >= 48 && oi == 4;
      const int orow = tl ? 160 * oP + 40 * (w & 1) + 16 : 160 * oP + 40 * oi + 24 * (w & 1);
      const char* src = (jks < 48 ? wd + (jks * 32 + (w & 1) * 16) * 1280 : wo + orow * 1280) + jG * 128;
      const unsigned vo = ((lane >> 3) + (lane >> 5) * (tl ? 76 : 0)) * 1280 + (lane & 7) * 16;
      asm volatile("global_load_dwordx4 %0, %1, %2" : "=v"(rt[0]) : "v"(vo), "s"(src) : "memory");
      asm volatile("global_load_dwordx4 %0, %1, %2" : "=v"(rt[1]) : "v"(vo), "s"(src + (tl ? 4 : 8) * 1280) : "memory");
    }
    if constexpr (K == K_SM) {
      asm volatile("s_waitcnt vmcnt(%5)" : "+v"(sx4), "+v"(sx1), "+v"(gen), "+v"(rt[0]), "+v"(rt[1]) : "i"(wait_mid(OPS[0], K, 3)) : "memory");
      tag = __builtin_amdgcn_readfirstlane(gen) * 0x9E3779B1u + 0x7F4A7C15u;
      sp0 = lo_f(__builtin_amdgcn_readfirstlane(sx4[0]));
      ss1 = 0.f; ss2 = 0.f;
    }
    if constexpr (K == K_SM || K == K_SM + 1) {
#pragma unroll
      for (int i = K == K_SM ? 0 : 2; i < (K == K_SM ? 2 : 5); ++i) {
        const unsigned u = i < 4 ? sx4[i] : sx1;
        const float d0 = lo_f(u) - sp0, d1 = hi_f(u) - sp0;
        ss1 += d0 + d1;
        ss2 = __builtin_fmaf(d0, d0, ss2); ss2 = __builtin_fmaf(d1, d1, ss2);
      }
    }
    if constexpr (K == K_SM + 3) {
      ss1 = dpp_add<0x111, 15>(ss1); ss2 = dpp_add<0x111, 15>(ss2); ss1 = dpp_add<0x112, 15>(ss1); ss2 = dpp_add<0x112, 15>(ss2);
      ss1 = dpp_add<0x114, 15>(ss1); ss2 = dpp_add<0x114, 15>(ss2); ss1 = dpp_add<0x118, 15>(ss1); ss2 = dpp_add<0x118, 15>(ss2);
      ss1 = dpp_add<0x142, 10>(ss1); ss2 = dpp_add<0x142, 10>(ss2); ss1 = dpp_add<0x143, 12>(ss1); ss2 = dpp_add<0x143, 12>(ss2);
    }
    if constexpr (K == K_SM + 4) {
      const float md = ss1 * (1.f / 640.f);
      const float var = __builtin_fmaxf(__builtin_fmaf(ss2, 1.f / 640.f, -md * md), 0.f);
      grn[0] = pk2(sp0 + md, __builtin_amdgcn_sqrtf(bfr(bfr(var) + 1e-5f)));
    }
    if constexpr (K == K_CP1 || K == K_CP2) {
      REFRESH();
      const bool on = lane < ((rgp < (K == K_CP2 ? 2 : 3) && pos < 5) ? 32 : 0);
      unsigned vo = on ? n * 16 : OOB, d = __float_as_uint(cs[0]);
      const unsigned so = CS_OFF + (g * 10 + (K == K_CP2 ? 6 + 2 * nlo : 3 * nlo) + rgp) * 2560 + (own >> 1) * 512 + (own & 1) * 8;
      if constexpr (K == K_CP1) { vo = lane == 63 ? GR_OFF + srow * 8 - so : vo; d = lane == 63 ? grn[0] : d; }
      st64(rws, vo, so, (u32x2){d, tag});
    }
    if constexpr (K == 15) {
      *(LAS bf16x8*)(size_t)(RTO + wb1 + l16) = rt[0];
      *(LAS bf16x8*)(size_t)(RTO + 8192 + wb1 + l16) = rt[1];
    }
    if constexpr (K == 16) {
      asm volatile("" ::: "memory");
      const unsigned a = RTO + wb1 + ((lane >> 2) & 3) * 128 + (lane >> 4) * 32 + (lane & 3) * 8;
      rt[0] = lds_trfrag(a, 512); rt[1] = lds_trfrag(a + 8192, 512);
      asm volatile("" : "+v"(rt[0]), "+v"(rt[1]) :: "memory");
      const int f = 4 * jG + (lane >> 4), ct = (f * 205) >> 10, c = f - 5 * ct;
      vs = jok ? (ct * (jks < 48 ? 240 : 100) + c) * 1024 + (lane & 15) * 16 : OOB;
    }
    if constexpr (K == K_ST0) {
      const unsigned so = (jks < 48 ? B2H_OFF + jks * 5120 : B2O_OFF + (jks - 48) * 5120) + (w & 1) * 512;
      st128(rws, vs, so, __builtin_bit_cast(u32x4, rt[0]));
      st128(rws, vs, so + 256, __builtin_bit_cast(u32x4, rt[1]));
    }
    if constexpr (K == 21) {
      REFRESH();
      const int r = lane >> 3, u = lane & 7, sw = ((r >> 1) & 1) << 2;
      gv2 = r * 3072 + ((u ^ sw) << 4) + (3 * p + 1 + odd) * 128;
      uv2 = r * 3072 + ((u ^ sw ^ 2) << 4) + (3 * p + 1 + odd) * 128;
    }
    if constexpr (K == 28) sdg = mkdesc(wg + (size_t)(8 * opq(w)) * 3072, 0x40000000u);
    if constexpr (K == 29) sdu = mkdesc(wu + (size_t)(8 * opq(w)) * 3072, 0x40000000u);
    if constexpr (K == K_GR)
      asm volatile("buffer_load_dwordx2 %0, %1, %2, %3 offen sc1" : "=v"(grn) : "v"(n * 8), "s"(sdw), "s"(GR_OFF + (128 * mt + 32 * rgp) * 8) : "memory");
    if constexpr (K == K_CS1) {
      if (w < 6)
        asm volatile("buffer_load_dwordx4 %0, %3, %5, %6 offen sc1\n\tbuffer_load_dwordx4 %1, %3, %5, %6 offen offset:1024 sc1\n\t"
                     "buffer_load_dwordx4 %2, %4, %5, %6 offen offset:2048 sc1"
                     : "=&v"(cl[0]), "=&v"(cl[1]), "=&v"(cl[2]) : "v"(l16), "v"(h ? OOB : l16), "s"(sdw), "s"(CS_OFF + (g * 10 + w) * 2560) : "memory");
    }
    if constexpr (K == K_CF1) {
      asm volatile("s_waitcnt vmcnt(%4)" : "+v"(grn), "+v"(cl[0]), "+v"(cl[1]), "+v"(cl[2]) : "i"(wait_mid(OPS[0], K, 325)) : "memory");
      if (w < 6) cs_fin(CS_OFF + (g * 10 + w) * 2560, EX1 + wb1);
    }
    if constexpr (K == K_PL0) {
      if (w == 7) asm volatile("buffer_load_dwordx4 %0, %1, %2, %3 offen sc1" : "=v"(pol) : "v"(l16), "s"(sdw), "s"(F0_OFF) : "memory");
    }
    if constexpr (K == K_CF1 + 1) {
      if (!__all(grn[1] == tag)) {
#pragma unroll 1
        for (unsigned sp = 0; sp < SPIN_CAP; ++sp) {
          __builtin_amdgcn_s_sleep(4);
          grn = ld64(rws, n * 8, GR_OFF + (128 * mt + 32 * rgp) * 8);
          if (__all(grn[1] == tag)) break;
        }
      }
      nm = ((grn[0] & 0xffffu) ^ 0x8000u) * 0x10001u;
      sdv = hi_f(grn[0]);
      rstd = __builtin_amdgcn_rcpf(sdv);
      gsc = -LOG2E * rstd;
    }
    if constexpr (K == 35) {
#pragma unroll
      for (int i = 0; i < 3; ++i) acs[i] = lds_rd128(EX1 + (3 * nlo + i) * 1024 + l16);
    }
    if constexpr (K >= 36 && K < 39) acc1[K - 36] = mfma32(acs[K - 36], bf8((u32x4){nm, nm, nm, nm}), acc1[K - 36]);
    if constexpr (K == 38) wcs = (rgp & 1) ? vopq(wa2[1]) : vopq(wa2[0]);
    if constexpr (K == K_CK0) {
      if (w == 7) {
        asm volatile("s_waitcnt vmcnt(%1)" : "+v"(pol) : "i"(cmin(wait_mid(OPS[0], K, 340), wait_mid(OPS[1], K, 340))) : "memory");
        auto ok = [&]() { return ((pol[0] ^ tag) | (pol[1] ^ tag) | (pol[2] ^ tag) | (pol[3] ^ tag)) == 0; };
        if (!__all(ok())) {
#pragma unroll 1
          for (unsigned sp = 0; sp < SPIN_CAP; ++sp) {
            __builtin_amdgcn_s_sleep(4);
            pol = ld128(rws, fresh_lane() * 16, F0_OFF);
            if (__all(ok())) break;
          }
        }
        if (b == 0) st32(rws, fresh_lane() == 0 ? 0u : OOB, GEN_OFF, tag);
      }
    }
    if constexpr (K == 39) {
      REFRESH();
      vs = h * 32768 + n * 16;
      vh = (n >> 4) * 1024 + h * 256 + (n & 15) * 16;
    }
    if constexpr (K == 40 || K == 41) qkv_st(acc1[0], odd ? 32 * p + 8 + 12 * nlo : 32 * p + 4 * nlo, K & 1);
    if constexpr (K >= 42 && K < 50) {
      constexpr int z = K - 42;
      if (odd) {
        if constexpr (K < 46) qkv_st(acc1[(K - 40) >> 1], 32 * p + 8 + 12 * nlo + 4 * ((K - 40) >> 1), K & 1);
      } else {
        gu2(acc1[1 + (z >> 2)], z & 3);
        if constexpr ((z & 3) == 3) gu_st(3 * p, 2 * nlo + (z >> 2));
      }
    }
    if constexpr (K == K_ROLE) {
      REFRESH();
      if constexpr (ROLE == 1) {
#pragma unroll
        for (int r = 0; r < 4; ++r) av[r] = l16 + ((r + rgp) & 3) * 1024;
        l16b = l16 + 4096;
        const int bb = opq(b), ct = (bb >> 3) & 7;
        kt1 = 0x76107610u >> (4 * ((ct + (bb >> 6)) & 3));
        kt2 = (unsigned)(0xBA985432BA985432ull >> (4 * ((ct + 2 * (bb >> 6)) & 7)));
        abase = A2H_OFF + ((4 * (bb & 7) + (bb >> 6)) * 48 + 12 * rgp) * 4096;
        bbase = B2H_OFF + (ct * 48 + 12 * rgp) * 5120;
      }
    }
    if constexpr (K > K_ROLE) {
      if constexpr (ROLE == 0) {
        if constexpr (K == K_PL1) pol1 = pol1_ld();
        if constexpr (K == K_CH1) st1 = pol1_ok() ? 1 : 0;
        if constexpr (K == K_CH1 + 1) { if (st1 == 0) pol1 = pol1_ld(); }
        if constexpr (K == K_CH2) { if (st1 == 0 && pol1_ok()) st1 = 2; }
        if constexpr (K >= K_CH1 && K < K_CH1 + 8) { if (st1 == 1) kv_piece(K - K_CH1); }
        if constexpr (K >= K_CH2 && K < K_CH2 + 6) { if (st1 == 2) { kv_piece(K - K_CH2); if constexpr (K >= K_CH2 + 4) kv_piece(K - K_CH2 + 2); } }
      } else {
        if constexpr (K == K_CS2) { cl[0] = ld128(rws, l16, CS_OFF + (g * 10 + 6 + rgp) * 2560); cl[1] = ld128(rws, l16 + 1024, CS_OFF + (g * 10 + 6 + rgp) * 2560); }
        if constexpr (K == K_CS2 + 1) cl[2] = ld128(rws, h ? OOB : l16, CS_OFF + (g * 10 + 6 + rgp) * 2560 + 2048);
        if constexpr (K == K_CF2) cs_fin(CS_OFF + (g * 10 + 6 + rgp) * 2560, EX2 + rgp * 1024);
        if constexpr (K >= K_BH && K < K_BH + 20) ldB((K - K_BH) / 5, (K - K_BH) % 5);
        if constexpr (K == K_PH1) pol1 = polh_ld();
        if constexpr (K == K_AH) {
          if (!__all(pol1 == tag)) {
#pragma unroll 1
            for (unsigned sp = 0; sp < SPIN_CAP; ++sp) {
              __builtin_amdgcn_s_sleep(8);
              if (__all(polh_ld() == tag)) break;
            }
          }
        }
        if constexpr (K >= K_AH) static_rng<16 * (K - K_AH) / (80 - K_AH), 16 * (K + 1 - K_AH) / (80 - K_AH)>([&](auto I) { ldA(I >> 2, I & 3); });
      }
    }
    if constexpr (K == 71) {
#pragma unroll
      for (int i = 0; i < 2; ++i) acs[i] = lds_rd128(EX2 + (2 * nlo + i) * 1024 + l16);
    }
    if constexpr (K == 72 || K == 73) acc2[K - 72] = mfma32(acs[K - 72], bf8((u32x4){nm, nm, nm, nm}), acc2[K - 72]);
    if constexpr (K == 74) {
      const unsigned o1 = w < 5 ? 0x3f803f80u : 0u;
      if (w < 6) *(LAS u32x4*)(size_t)((w < 5 ? ONES + wb1 : MISC) + l16) = (u32x4){o1, o1, o1, o1};
    }
    SB();
  };
  rd(IC(0));
  static_rng<0, 52>([&](auto KK) { step(KK, IC(2), IC(2)); });

  auto epi2 = [&]() {
    STAMP(4);
    asm volatile("s_nop 7\n\ts_nop 7" : "+v"(acc2[0]), "+v"(acc2[1]));
#pragma unroll
    for (int i = 0; i < 2; ++i) {
#pragma unroll
      for (int z = 0; z < 4; ++z) gu2(acc2[i], z);
      gu_st(3 * p + 1 + odd, 2 * nlo + i);
    }
    SB();
  };
#define TAIL_COORDS \
  REFRESH(); \
  const int bb = opq(b), ct = (bb >> 3) & 7, rg = 4 * (bb & 7) + (bb >> 6); \
  const unsigned f2w = F2_OFF + (2 * mt + (rgp >> 1)) * 256 + (g * 4 + (rgp & 1) * 2 + nlo) * 4

  if (nlo == 0) {
    static_rng<52, 80>([&](auto KK) { step(KK, IC(2), IC(0)); });
    epi2();
    TAIL_COORDS;
    const int e = (bb >> 3) & 1, QB = bb >> 5, pair = 2 * (bb & 7) + ((bb >> 4) & 1);
    const unsigned ovu = (n >> 4) * 20480 + (n & 15) * 16 + h * 256, ovm = h ? OOB : (n >> 4) * 20480 + (n & 15) * 8;
    const unsigned oso = A2O_OFF + (16 * QB + 4 * rgp) * 20480 + (pair >> 2) * 5120;
    auto o_st = [&](int a, u32x4 u, u32x2 m) {
      st128(rws, ovu, oso + 2 * a * 20480 + (pair & 3) * 1024 + e * 512, u);
      st64(rws, ovm, oso + 2 * a * 20480 + 4096 + (2 * (pair & 3) + e) * 128, m);
    };
    if (st1 == 0) {
#pragma unroll 1
      for (unsigned sp = 0; sp < SPIN_CAP; ++sp) {
        pol1 = pol1_ld();
        if (pol1_ok()) break;
        __builtin_amdgcn_s_sleep(4);
      }
      static_for<8>([&](auto I) { kv_piece(I); });
      wait_vm<0>();
    } else wait_vm<2>();
    wait_l0();
    asm volatile("" :: "v"(pfd));
    barrier();
    STAMP(5);
    __builtin_amdgcn_s_setprio(3);
    const int jmin = QB ? 0 : 8 - 2 * rgp;
    const unsigned ka1 = KO + (e + h) * US + (64 * rgp + n) * 16;
    const unsigned ka2 = KO + (e ? 8 : 2 * US) + (64 * rgp + n) * 16;
    unsigned va;
    {
      const int g1 = (lane >> 4) & 1, q = (lane >> 2) & 3, pp = lane & 3;
      const unsigned um = (e + (pp & 1)) * US + (pp >> 1) * 8, ut = e ? 8 : 2 * US;
      unsigned uo = VO + (g1 ? ut : um) + rgp * 1024;
      asm volatile("" : "+v"(uo));
      va = ((g1 * pp) ? ONES : uo) + (4 * h + q) * 16;
    }
    auto rdK1 = [&](int j) { return lds_rd128(ka1 + j * 512); };
    auto rdK2 = [&](int j) { return *(LAS u32x2*)(size_t)(ka2 + j * 512); };
    auto rdV = [&](int j, int sh) { return lds_trfrag(va + j * 512 + sh * 256, 128); };
    bf16x8 k1[2], vf[2][2]; u32x2 k2[2];
    k1[1] = rdK1(9); k2[1] = rdK2(9); k1[0] = rdK1(8); k2[0] = rdK2(8);
    vf[1][0] = rdV(9, 0); vf[1][1] = rdV(9, 1);
    f32x16 cc[2], o[2], sc[2][2];
#pragma unroll
    for (int a = 1; a >= 0; --a) {
      const u32x4 ku = __builtin_bit_cast(u32x4, k1[a]);
      float t = 0.f;
#pragma unroll
      for (int i = 0; i < 6; ++i)
        t = __builtin_amdgcn_fdot2_f32_bf16(__builtin_bit_cast(bf16x2, i < 4 ? ku[i] : k2[a][i - 4]),
                                            __builtin_bit_cast(bf16x2, i < 4 ? q1[a][i] : q2[a][i - 4]), t, false);
      auto r = __builtin_amdgcn_permlane32_swap(__float_as_uint(t), __float_as_uint(t), false, false);
      cc[a] = (f32x16)(-(__uint_as_float(r[0]) + __uint_as_float(r[1])));
      o[a] = (f32x16)(0.f);
    }
    sc[1][1] = mfma8(k2[1], q2[1], mfma32(k1[1], bf8(q1[1]), cc[1]));
    unsigned mk[8];
#pragma unroll
    for (int r = 0; r < 8; ++r) {
      const int c = n - 4 * h - ((2 * r & 3) + 8 * (r >> 1));
      mk[r] = (c >= 0 ? 0xffffu : 0u) | (c >= 1 ? 0xffff0000u : 0u);
    }
    SB();
    auto expk = [&](const f32x16& s, int sh, int mode) {
      u32x4 pu;
#pragma unroll
      for (int r = 0; r < 4; ++r) {
        pu[r] = pk2(__builtin_amdgcn_exp2f(s[8 * sh + 2 * r]), __builtin_amdgcn_exp2f(s[8 * sh + 2 * r + 1]));
        if (mode == 1) pu[r] &= mk[4 * sh + r];
        if (mode == 2) pu[r] &= ~mk[4 * sh + r];
      }
      return bf8(pu);
    };
    u32x2 xres[5];
    static_for<10>([&](auto TT) {
      constexpr int J = 9 - TT, cu = J & 1, nx = cu ^ 1;
      constexpr bool hasA = J <= 8, hasB = J >= 1, nA = J >= 1, nB = J >= 2;
      constexpr int mA = J == 8 ? 1 : J == 0 ? 2 : 0, mB = J == 9 ? 1 : J == 1 ? 2 : 0;
      if (J >= jmin) {
        bf16x8 pp;
        if constexpr (nB) sc[nx][1] = mfma32(k1[nx], bf8(q1[1]), cc[1]);
        if constexpr (hasA) pp = expk(sc[cu][0], 0, mA);
        if constexpr (nA) sc[nx][0] = mfma32(k1[nx], bf8(q1[0]), cc[0]);
        if constexpr (J >= 2) k1[cu] = rdK1(J - 2);
        if constexpr (J >= 1) vf[nx][0] = rdV(J - 1, 0);
        SB();
        if constexpr (hasA) { o[0] = mfma32(vf[cu][0], pp, o[0]); pp = expk(sc[cu][0], 1, mA); }
        if constexpr (nB) sc[nx][1] = mfma8(k2[nx], q2[1], sc[nx][1]);
        if constexpr (J >= 1) vf[nx][1] = rdV(J - 1, 1);
        SB();
        if constexpr (hasA) o[0] = mfma32(vf[cu][1], pp, o[0]);
        if constexpr (hasB) pp = expk(sc[cu][1], 0, mB);
        if constexpr (nA) sc[nx][0] = mfma8(k2[nx], q2[0], sc[nx][0]);
        if constexpr (J >= 2) k2[cu] = rdK2(J - 2);
        SB();
        if constexpr (hasB) {
          o[1] = mfma32(vf[cu][0], pp, o[1]); pp = expk(sc[cu][1], 1, mB);
          o[1] = mfma32(vf[cu][1], pp, o[1]);
        }
        SB();
      }
      if constexpr (J == 8) {
        wait_vm<0>();
        st32(rws, lane == 0 ? 0u : OOB, f2w, tag);
        SB();
      }
    });
    STAMP(6);
    wait_vm<0>();
#pragma unroll
    for (int a = 0; a < 2; ++a) {
      float amax = 0.f;
#pragma unroll
      for (int r = 0; r < 12; ++r) amax = __builtin_fmaxf(amax, __builtin_fabsf(o[a][r]));
      const bool okv = (o[a][12] >= 9.0e-13f) & (o[a][12] <= 1.0e30f) & (amax <= 3.0e38f);
      if (!__all(okv)) {
        float mx = -__builtin_inff();
#pragma unroll 1
        for (int pass = 0; pass < 2; ++pass) {
          o[a] = (f32x16)(0.f);
#pragma unroll 1
          for (int j = (jmin > a ? jmin : a); j < 9 + a; ++j) {
            f32x16 s = mfma8(rdK2(j), q2[a], mfma32(rdK1(j), bf8(q1[a]), (f32x16)(0.f)));
            const unsigned fl = j == a ? ~0u : 0u, al = (j == a || j == 8 + a) ? 0u : ~0u;
#pragma unroll
            for (int r = 0; r < 8; ++r) {
              const int m = (int)((mk[r] ^ fl) | al);
              s[2 * r] = (m & 1) ? s[2 * r] : -__builtin_inff();
              s[2 * r + 1] = m < 0 ? s[2 * r + 1] : -__builtin_inff();
            }
            if (pass == 0) {
#pragma unroll
              for (int r = 0; r < 16; ++r) mx = __builtin_fmaxf(mx, s[r]);
            } else {
#pragma unroll
              for (int r = 0; r < 16; ++r) s[r] -= mx;
              o[a] = mfma32(rdV(j, 0), expk(s, 0, 0), o[a]);
              o[a] = mfma32(rdV(j, 1), expk(s, 1, 0), o[a]);
            }
          }
          if (pass == 0) {
            auto t2 = __builtin_amdgcn_permlane32_swap(__float_as_uint(mx), __float_as_uint(mx), false, false);
            mx = __builtin_fmaxf(__uint_as_float(t2[0]), __uint_as_float(t2[1]));
          }
        }
      }
      const float il = __builtin_amdgcn_rcpf(o[a][12]);
      auto ov = [&](int i) { return __builtin_elementwise_min(pk2(o[a][2 * i] * il, o[a][2 * i + 1] * il), 0xfffffffeu); };
      o_st(a, (u32x4){ov(0), ov(1), ov(2), ov(3)}, (u32x2){ov(4), ov(5)});
    }
    SB();
    lds_inc(CNTA);
    const u32x4 blast = ld128(rws, l16, B2O_OFF + ct * 102400 + 99 * 1024);
    wait_vm<1>();
    st32(rws, lane == 0 ? 0u : OOB, F3_OFF + (4 * QB + rgp) * 128 + (2 * pair + e) * 4, tag);
    TSTAMP(7);
    lds_wait(CNTD, 4);
    TSTAMP(8);
    bf16x8 bo[4][25];
    unsigned ba[4];
#pragma unroll
    for (int P = 0; P < 4; ++P) ba[P] = BO + ((P + ct) & 3) * 25600 + l16;
    auto rdB = [&](int P, int i) {
      bo[P][i] = lds_rd128(ba[P] + i * 1024);
      if (i == 24) bo[P][i] = ((P + ct) & 3) == 3 ? bf8(blast) : bo[P][i];
    };
    static_for<25>([&](auto I) { rdB(0, I); });
    SB();
    lds_wait(CNTL, 4);
    if (rgp == 0) {
#pragma unroll 1
      for (unsigned sp = 0; sp < SPIN_CAP; ++sp) {
        const unsigned p3 = ld32(rws, n * 4, F3_OFF + rg * 128);
        if (__all(p3 == tag)) break;
        __builtin_amdgcn_s_sleep(PSL);
      }
      *(volatile LAS unsigned*)(size_t)RDY3 = 1u;
    } else lds_wait(RDY3, 1);
    SB();
    TSTAMP(9);
    u32x4 ao[4][5];
    auto ldO = [&](int P) {
      const unsigned so = A2O_OFF + (4 * rg + rgp) * 20480 + ((P + ct) & 3) * 5120;
#pragma unroll
      for (int i = 0; i < 4; ++i) ao[P][i] = ld128(rws, l16 + i * 1024, so);
      const u32x2 t0 = ld64(rws, lane * 8, so + 4096), t1 = ld64(rws, lane * 8 + 512, so + 4096);
      ao[P][4] = (u32x4){t0[0], t0[1], t1[0], t1[1]};
    };
    auto okO = [&](int P) {
      unsigned m = 0;
#pragma unroll
      for (int i = 0; i < 5; ++i) {
        m = __builtin_elementwise_max(__builtin_elementwise_max(m, ao[P][i][0]), ao[P][i][1]);
        m = __builtin_elementwise_max(__builtin_elementwise_max(m, ao[P][i][2]), ao[P][i][3]);
      }
      return __all(m != ~0u);
    };
    static_for<4>([&](auto P) { ldO(P); });
    SB();
    f32x4 acc[5];
#pragma unroll
    for (int c = 0; c < 5; ++c) {
      acc[c] = (f32x4)(0.f);
    }
    static_for<20>([&](auto II) {
      constexpr int P = II / 5, i = II % 5;
      if constexpr (P < 3) static_for<5>([&](auto C) { rdB(P + 1, 5 * i + C); });
#pragma unroll
      for (int c = 0; c < 5; ++c) acc[c] = mfma16(bo[P][5 * i + c], bf8(ao[P][i]), acc[c]);
      SB();
    });
    TSTAMP(10);
    const unsigned oo = (64 * rg + 16 * rgp + (lane & 15)) * 1280 + ct * 160 + (lane >> 4) * 8;
    lds_wait(RDYB + rgp * 4, 1);
#pragma unroll
    for (int c = 0; c < 5; ++c) {
      f32x4 t = acc[c] + lds_rdf4(rgp * 15360 + c * 1024 + l16);
      *(u32x2*)(out + oo + c * 32) = (u32x2){pk2(t[0], t[1]), pk2(t[2], t[3])};
    }
    STAMP(15);
    STAMP_FLUSH(0);
    asm volatile("fused_lx:");
    __builtin_amdgcn_endpgm();
  } else {
    static_rng<52, 80>([&](auto KK) { step(KK, IC(2), IC(1)); });
    epi2();
    wait_l0();
    barrier();
    STAMP(5);
    TAIL_COORDS;
    asm volatile("" :: "v"(aq[3][3]), "v"(aq[3][2]), "v"(aq[3][1]), "v"(aq[3][0]), "v"(bq[3][4]));
    SB();
    f32x4 acc[4][5];
    auto slot = [&](int s) {
#pragma unroll
      for (int r = 0; r < 4; ++r)
#pragma unroll
        for (int c = 0; c < 5; ++c) acc[r][c] = mfma16(bf8(bq[s][c]), bf8(aq[s][r]), s == 0 ? (f32x4)(0.f) : acc[r][c]);
    };
    const unsigned bsrc = B2O_OFF + ct * 102400 + rgp * 25600, bdst = BO + rgp * 25600;
    auto dmaB = [&](int i) {
      if (i < 6) dma_lin<4>(l16, sdw, bsrc + i * 4096, bdst + i * 4096);
      else dma_lin<1>(l16, sdw, bsrc + (rgp < 3 ? 24576 : 23552), bdst + (rgp < 3 ? 24576 : 23552));
    };
    dmaB(0); dmaB(1); SB(); slot(0); SB();
    static_for<5>([&](auto C) { ldB(4, C); });
    dmaB(2); dmaB(3); SB(); slot(1); SB();
    static_for<5>([&](auto C) { ldB(5, C); });
    wait_vm<26>();
    asm volatile("" :: "v"(pfd));
    st32(rws, lane == 0 ? 0u : OOB, f2w, tag);
    dmaB(4); dmaB(5); dmaB(6); SB(); slot(2); SB();
    static_for<10>([&](auto C) { ldB(6 + C / 5, C % 5); });
    SB(); slot(3); SB();
    STAMP(6);
    unsigned p2 = ld32(rws, (lane & 15) * 4, F2_OFF + rg * 256 + rgp * 64);
    wait_vm<11>();
    lds_inc(CNTD);
    SB();
    if (!__all(p2 == tag)) {
#pragma unroll 1
      for (unsigned sp = 0; sp < SPIN_CAP; ++sp) {
        p2 = ld32(rws, (lane & 15) * 4, F2_OFF + rg * 256 + rgp * 64);
        if (__all(p2 == tag)) break;
        __builtin_amdgcn_s_sleep(2);
      }
    }
    SB();
    TSTAMP(7);
    static_for<4>([&](auto I) { ldA(4, I); });
    SB();
    asm volatile("" :: "v"(aq[4][0]));
    static_for<12>([&](auto I) { ldA(5 + I / 4, I % 4); });
    SB();
    u32x2 xres[5];
    static_rng<4, 12>([&](auto SS) {
      constexpr int s = SS;
      slot(s); SB();
      if constexpr (s + 4 < 12) static_for<4>([&](auto I) { ldA(s + 4, I); });
      if constexpr (s + 4 < 12) static_for<5>([&](auto C) { ldB(s + 4, C); });
      if constexpr (s == WAITY) lds_inc(CNTL);
      if constexpr (s == 9) {
        const unsigned oo = (64 * rg + 16 * rgp + (lane & 15)) * 1280 + ct * 160 + (lane >> 4) * 8;
#pragma unroll
        for (int c = 0; c < 5; ++c) xres[c] = *(const u32x2*)(x + oo + c * 32);
      }
      SB();
    });
    TSTAMP(8);
    lds_wait(CNTA, 4);
#pragma unroll
    for (int d = 1; d < 4; ++d) {
      const unsigned a = ((rgp + d) & 3) * 15360 + (d - 1) * 5120 + l16;
#pragma unroll
      for (int c = 0; c < 5; ++c) *(LAS f32x4*)(size_t)(a + c * 1024) = acc[d][c];
    }
    lds_inc(CNTY);
    lds_wait(CNTY, 4);
    TSTAMP(9);
    const unsigned a = rgp * 15360 + l16;
    f32x4 t[5];
#pragma unroll
    for (int c = 0; c < 5; ++c) {
      t[c] = (acc[0][c] + lds_rdf4(a + c * 1024)) + (lds_rdf4(a + 5120 + c * 1024) + lds_rdf4(a + 10240 + c * 1024));
      t[c] += (f32x4){lo_f(xres[c][0]), hi_f(xres[c][0]), lo_f(xres[c][1]), hi_f(xres[c][1])};
    }
    SB();
#pragma unroll
    for (int c = 0; c < 5; ++c) *(LAS f32x4*)(size_t)(a + c * 1024) = t[c];
    asm volatile("" ::: "memory");
    *(volatile LAS unsigned*)(size_t)(RDYB + rgp * 4) = 1u;
    TSTAMP(10);
  }
  STAMP(15);
  STAMP_FLUSH(0);
  asm volatile("fused_ly:");
}

extern "C" void kernel_launch(void* const* d_in, const int* in_sizes, int n_in, void* d_out, int out_size,
                              void* d_ws, size_t ws_size, hipStream_t stream) {
  (void)hipFuncSetAttribute((const void*)fused, hipFuncAttributeMaxDynamicSharedMemorySize, 163840);
  hipLaunchKernelGGL(fused, dim3(256), dim3(512), 163840, stream, (const char*)d_in[0], (const char*)d_in[1],
                     (const char*)d_in[2], (const char*)d_in[4], (const char*)d_in[5], (const char*)d_in[6],
                     (char*)d_ws, (char*)d_out);
}
```
